```python
import torch

def _softmax(x, axis=-1):
    return torch.softmax(x, dim=axis)

def _gelu(x):
    return torch.nn.functional.gelu(x, approximate='tanh')

def _silu(x):
    return torch.nn.functional.silu(x)

def _sigmoid(x):
    return torch.sigmoid(x)

def _layernorm(x, axis=-1, eps=1e-05):
    m = x.mean(dim=axis, keepdim=True)
    v = x.var(dim=axis, keepdim=True, unbiased=False)
    return (x - m) / torch.sqrt(v + eps)

def _scatter_add(tgt, idx, src, axis=0):
    return tgt.clone().index_add_(axis, idx, src)

def _segment_sum(src, seg, n):
    out = torch.zeros((n,) + src.shape[1:], dtype=src.dtype)
    return out.index_add_(0, seg, src)

def _pad(x, pw):
    flat = tuple((v for pair in reversed(pw) for v in pair))
    return torch.nn.functional.pad(x, flat)
kernel_name = 'sequential_preLN__memory_aug_recurrent_mem_tr__glu_gated_reglu__prompt'

def setup_inputs():

    _w = lambda a: (lambda t: t.to(torch.bfloat16) if t.is_floating_point() else t)(torch.as_tensor(a))

    def _A_setup():
        B, S, Dm = (1, 2048, 640)
        H, Dh = (32, 128)
        M, seg = (8, 32)
        seg = min(seg, S)
        S = S // seg * seg
        return {'x': _w((lambda *s: torch.randn(*s, dtype=torch.bfloat16) * 0.05)(B, S, Dm)), 'mem0': _w((lambda *s: torch.randn(*s, dtype=torch.bfloat16) * 0.2)(B, M, Dm)), 'Wq': _w((lambda *s: torch.randn(*s, dtype=torch.bfloat16) * 1.6)(Dm, H, Dh) * Dm ** (-0.5)), 'Wk': _w((lambda *s: torch.randn(*s, dtype=torch.bfloat16) * 1.6)(Dm, H, Dh) * Dm ** (-0.5)), 'Wv': _w((lambda *s: torch.randn(*s, dtype=torch.bfloat16) * 0.2)(Dm, H, Dh) * Dm ** (-0.5)), 'Wo': _w((lambda *s: torch.randn(*s, dtype=torch.bfloat16) * 0.2)(H, Dh, Dm) * (H * Dh) ** (-0.5)), 'seg_len': seg}
    _A_in = _A_setup()

    def _B_setup():
        B, S, D, F = (1, 2048, 640, 1728)
        return {'x': _w((lambda *s: torch.randn(*s, dtype=torch.bfloat16) * 0.05)(B, S, D)), 'W_gate': _w((lambda *s: torch.randn(*s, dtype=torch.bfloat16) * 0.2)(D, F) * (2.0 / D) ** 0.5), 'W_up': _w((lambda *s: torch.randn(*s, dtype=torch.bfloat16) * 0.2)(D, F) * (2.0 / D) ** 0.5), 'W_down': _w((lambda *s: torch.randn(*s, dtype=torch.bfloat16) * 0.2)(F, D) * (1.0 / F) ** 0.5)}
    _B_in = _B_setup()
    return {'A_x': _A_in['x'], 'A_mem0': _A_in['mem0'], 'A_Wq': _A_in['Wq'], 'A_Wk': _A_in['Wk'], 'A_Wv': _A_in['Wv'], 'A_Wo': _A_in['Wo'], 'A_seg_len': _A_in['seg_len'], 'B_x': _B_in['x'], 'B_W_gate': _B_in['W_gate'], 'B_W_up': _B_in['W_up'], 'B_W_down': _B_in['W_down']}

def reference(A_x, A_mem0, A_Wq, A_Wk, A_Wv, A_Wo, A_seg_len, B_x, B_W_gate, B_W_up, B_W_down):

    def _A_ref(x, mem0, Wq, Wk, Wv, Wo, seg_len):
        B, S, Dm = x.shape
        H, Dh = (Wq.shape[1], Wq.shape[2])
        M = mem0.shape[1]
        scale = Dh ** (-0.5)
        n_seg = S // seg_len
        L = M + seg_len
        idx = torch.arange(L)
        mask = (idx[:, None] < M) | (idx[:, None] >= idx[None, :])
        mem = mem0
        outs = []
        for s in range(n_seg):
            xs = x[:, s * seg_len:(s + 1) * seg_len, :].clone()
            h = torch.concatenate([mem, xs], axis=1)
            q = torch.einsum('btd,dhe->bhte', h, Wq)
            k = torch.einsum('btd,dhe->bhte', h, Wk)
            v = torch.einsum('btd,dhe->bhte', h, Wv)
            scores = torch.einsum('bhqe,bhke->bhqk', q, k) * scale
            scores = torch.where(mask[None, None], scores, -1000000000.0)
            p = _softmax(scores, axis=-1)
            o = torch.einsum('bhqk,bhke->bhqe', p, v)
            o = torch.einsum('bhte,hed->btd', o, Wo)
            mem = o[:, :M, :].clone()
            outs.append(o[:, M:, :])
        return torch.concatenate(outs, axis=1)

    def _B_ref(x, W_gate, W_up, W_down):
        gate = torch.einsum('bsd,df->bsf', x, W_gate)
        up = torch.einsum('bsd,df->bsf', x, W_up)
        gate = gate * (lambda x, dt: x.to(dt))(gate > 0, gate.dtype)
        h = gate * up
        return torch.einsum('bsf,fd->bsd', h, W_down)
    x = A_x
    h = x + _A_ref(_layernorm(x, axis=-1), A_mem0, A_Wq, A_Wk, A_Wv, A_Wo, A_seg_len)
    h2 = h + _B_ref(_layernorm(h, axis=-1), B_W_gate, B_W_up, B_W_down)
    return h2
```

```cpp
#include <hip/hip_runtime.h>
#include <hip/hip_bf16.h>
#include <stdint.h>

typedef __bf16 bf16x8 __attribute__((ext_vector_type(8)));
typedef __bf16 bf16x16 __attribute__((ext_vector_type(16)));
typedef __bf16 bf16x4 __attribute__((ext_vector_type(4)));
typedef float f32x4 __attribute__((ext_vector_type(4)));
typedef unsigned u32x4 __attribute__((ext_vector_type(4)));
typedef unsigned u32x2 __attribute__((ext_vector_type(2)));
typedef short s16x4 __attribute__((ext_vector_type(4)));
typedef __attribute__((address_space(3))) char* lds_ptr;
typedef unsigned short u16;

#define DEVI __device__ __forceinline__

constexpr int S_ = 2048, D_ = 640, H_ = 32, E_ = 128, HE = 4096, F_ = 1728, NSEG = 64;
constexpr size_t OFF_XN = 0;
constexpr size_t OFF_Q = OFF_XN + (size_t)S_ * D_ * 2;
constexpr size_t OFF_K = OFF_Q + (size_t)S_ * HE * 2;
constexpr size_t OFF_U = OFF_K + (size_t)S_ * HE * 2;
constexpr size_t OFF_KM = OFF_U + (size_t)NSEG * 40 * 32 * 1024;
constexpr size_t OFF_PT = OFF_KM + (size_t)NSEG * 32 * 4096;
constexpr size_t OFF_H = OFF_PT + (size_t)NSEG * 32 * 3072;
constexpr size_t OFF_HN = OFF_H + (size_t)S_ * D_ * 2;
constexpr size_t OFF_ACT = OFF_HN + (size_t)S_ * D_ * 2;
constexpr size_t OFF_X = OFF_ACT + (size_t)S_ * F_ * 2;
constexpr size_t MEM_STEP = 10240, P_STEP = 20480, VW_STEP = 327680;
constexpr size_t OFF_MEM = OFF_X;
constexpr size_t OFF_P = OFF_MEM + NSEG * MEM_STEP;
constexpr size_t OFF_VW = OFF_P + NSEG * P_STEP;
constexpr size_t OFF_END = OFF_VW + NSEG * VW_STEP;
static_assert(OFF_X % 256 == 0 && OFF_U % 256 == 0 && OFF_KM % 256 == 0 && OFF_PT % 256 == 0, "align");
static_assert(OFF_END <= (size_t)256 * 1024 * 1024, "ws");

typedef __bf16 bf16x2 __attribute__((ext_vector_type(2)));
typedef float f32x2 __attribute__((ext_vector_type(2)));
DEVI unsigned cvtpk(float lo, float hi) { const f32x2 v = {lo, hi}; return __builtin_bit_cast(unsigned, __builtin_convertvector(v, bf16x2)); }
DEVI float bflo(unsigned u) { return __uint_as_float(u << 16); }
DEVI float bfhi(unsigned u) { return __uint_as_float(u & 0xffff0000u); }
DEVI float rbf(float x) { return bflo(cvtpk(x, x)); }
DEVI unsigned nosent(unsigned u) { return u < 0xFFFFFFFEu ? u : 0xFFFFFFFEu; }
DEVI void glds16(const void* g, unsigned lds_base) {
  unsigned sv;
  asm volatile("s_mov_b32 %0, m0\n\ts_mov_b32 m0, %2\n\ts_nop 0\n\tglobal_load_lds_dwordx4 %1, off\n\ts_mov_b32 m0, %0" : "=&s"(sv) : "v"(g), "s"(lds_base) : "memory");
}
DEVI s16x4 trread(unsigned a) { return __builtin_amdgcn_ds_read_tr16_b64_v4i16((__attribute__((address_space(3))) s16x4*)(uintptr_t)a); }
DEVI bf16x8 cat8(s16x4 a, s16x4 b) { return __builtin_bit_cast(bf16x8, __builtin_shufflevector(a, b, 0, 1, 2, 3, 4, 5, 6, 7)); }
DEVI u32x4 ldsr128(unsigned a) { return *(__attribute__((address_space(3))) u32x4*)(uintptr_t)a; }
DEVI u32x2 ldsr64(unsigned a) { return *(__attribute__((address_space(3))) u32x2*)(uintptr_t)a; }
DEVI void ldsw128(unsigned a, u32x4 v) { *(__attribute__((address_space(3))) u32x4*)(uintptr_t)a = v; }
DEVI void ldsw64(unsigned a, u32x2 v) { *(__attribute__((address_space(3))) u32x2*)(uintptr_t)a = v; }
DEVI bf16x8 asb8(u32x4 v) { return __builtin_bit_cast(bf16x8, v); }
#define MFMA16(a, b, c) __builtin_amdgcn_mfma_f32_16x16x32_bf16(a, b, c, 0, 0, 0)
#define SMF(a, b, c, idx) __builtin_amdgcn_smfmac_f32_16x16x64_bf16(a, b, c, idx, 0, 0)
#define BAR_ALL() asm volatile("s_waitcnt vmcnt(0) lgkmcnt(0)\n\ts_barrier" ::: "memory")
#define BAR_LDS() asm volatile("s_waitcnt lgkmcnt(0)\n\ts_barrier" ::: "memory")
DEVI float xmax16(float x) { auto r = __builtin_amdgcn_permlane16_swap(__float_as_uint(x), __float_as_uint(x), false, false); return fmaxf(__uint_as_float(r[0]), __uint_as_float(r[1])); }
DEVI float xmax32(float x) { auto r = __builtin_amdgcn_permlane32_swap(__float_as_uint(x), __float_as_uint(x), false, false); return fmaxf(__uint_as_float(r[0]), __uint_as_float(r[1])); }
DEVI float xsum16(float x) { auto r = __builtin_amdgcn_permlane16_swap(__float_as_uint(x), __float_as_uint(x), false, false); return __uint_as_float(r[0]) + __uint_as_float(r[1]); }
DEVI float xsum32(float x) { auto r = __builtin_amdgcn_permlane32_swap(__float_as_uint(x), __float_as_uint(x), false, false); return __uint_as_float(r[0]) + __uint_as_float(r[1]); }
DEVI float pairsum32(float a, float b) { auto r = __builtin_amdgcn_permlane32_swap(__float_as_uint(a), __float_as_uint(b), false, false); return __uint_as_float(r[0]) + __uint_as_float(r[1]); }
DEVI void st_sc1_x4(void* p, u32x4 v) { asm volatile("global_store_dwordx4 %0, %1, off sc1\n\ts_nop 1" ::"v"(p), "v"(v) : "memory"); }
DEVI void st_sc1_x2(void* p, u32x2 v) { asm volatile("global_store_dwordx2 %0, %1, off sc1\n\ts_nop 1" ::"v"(p), "v"(v) : "memory"); }
DEVI void st_sc1_x1(void* p, unsigned v) { asm volatile("global_store_dword %0, %1, off sc1\n\ts_nop 1" ::"v"(p), "v"(v) : "memory"); }
DEVI unsigned umax4(u32x4 v) { return max(max(v[0], v[1]), max(v[2], v[3])); }

DEVI void ln_row(const unsigned* __restrict__ src, unsigned* __restrict__ dst, int lane) {
  unsigned v[5]; float f[10]; float s = 0.f;
#pragma unroll
  for (int i = 0; i < 5; ++i) { v[i] = src[lane + 64 * i]; f[2 * i] = bflo(v[i]); f[2 * i + 1] = bfhi(v[i]); s += f[2 * i] + f[2 * i + 1]; }
#pragma unroll
  for (int o = 1; o < 64; o <<= 1) s += __shfl_xor(s, o);
  const float mean = s * (1.0f / 640.0f);
  float q = 0.f;
#pragma unroll
  for (int i = 0; i < 10; ++i) { float d = f[i] - mean; q += d * d; }
#pragma unroll
  for (int o = 1; o < 64; o <<= 1) q += __shfl_xor(q, o);
  const float mb = rbf(mean), vb = rbf(q * (1.0f / 640.0f));
  const float sd = rbf(sqrtf(rbf(vb + 1e-05f)));
#pragma unroll
  for (int i = 0; i < 5; ++i) { float a = rbf(f[2 * i] - mb), b = rbf(f[2 * i + 1] - mb); dst[lane + 64 * i] = cvtpk(a / sd, b / sd); }
}

constexpr int FILL_BLOCKS = 1024;
__global__ __launch_bounds__(256) void prep_kernel(const unsigned* __restrict__ x, const u16* __restrict__ mem0, char* __restrict__ ws) {
  const int b = blockIdx.x, t = threadIdx.x;
  if (b < 512) {
    const int row = b * 4 + (t >> 6);
    ln_row(x + (size_t)row * 320, (unsigned*)(ws + OFF_XN) + (size_t)row * 320, t & 63);
  } else if (b < 512 + FILL_BLOCKS) {
    u32x4* p = (u32x4*)(ws + OFF_MEM + MEM_STEP);
    const size_t n = (OFF_END - OFF_MEM - MEM_STEP) / 16;
    const u32x4 s = {~0u, ~0u, ~0u, ~0u};
    for (size_t i = (size_t)(b - 512) * 256 + t; i < n; i += (size_t)FILL_BLOCKS * 256) p[i] = s;
  } else {
    for (int i = t; i < 640; i += 256) {
      const int ct = i >> 4, bb = (i >> 3) & 1, r = i & 7;
      u32x4 v = *(const u32x4*)(mem0 + r * 640 + ct * 16 + bb * 8);
      v[0] = nosent(v[0]); v[1] = nosent(v[1]); v[2] = nosent(v[2]); v[3] = nosent(v[3]);
      *(u32x4*)(ws + OFF_MEM + (size_t)i * 16) = v;
    }
  }
}
__global__ __launch_bounds__(256) void ln2_kernel(char* __restrict__ ws) {
  const int row = blockIdx.x * 4 + (threadIdx.x >> 6);
  ln_row((const unsigned*)(ws + OFF_H) + (size_t)row * 320, (unsigned*)(ws + OFF_HN) + (size_t)row * 320, threadIdx.x & 63);
}

template <int BM>
DEVI void stageA(const u16* __restrict__ A, int lda, int row0, int k0, unsigned lds, int w, int lane) {
#pragma unroll
  for (int j = 0; j < BM / 32; ++j) {
    const int blk = w * (BM / 32) + j, pos = blk * 64 + lane, row = pos >> 3, ch = (pos & 7) ^ (row & 7);
    glds16(A + (size_t)(row0 + row) * lda + k0 + ch * 8, lds + blk * 1024);
  }
}
DEVI int swzB(int row) { return ((row & 3) << 2) | ((row >> 2) & 3); }
DEVI void stageB(const u16* __restrict__ B0, const u16* __restrict__ B1, int ldb, int k0, unsigned lds, int w, int lane) {
#pragma unroll
  for (int j = 0; j < 4; ++j) {
    const int blk = w * 4 + j, pos = blk * 64 + lane, row = pos >> 4, ch = (pos & 15) ^ swzB(row);
    const u16* src = (ch < 8 ? B0 + ch * 8 : B1 + (ch - 8) * 8) + (size_t)(k0 + row) * ldb;
    glds16(src, lds + blk * 1024);
  }
}
DEVI bf16x8 fragA(unsigned lds, int mt, int ks, int lane) {
  const int row = mt * 16 + (lane & 15), ch = 4 * ks + (lane >> 4);
  return asb8(ldsr128(lds + row * 128 + ((ch ^ (row & 7)) << 4)));
}
DEVI bf16x8 fragB(unsigned lds, int nt, int ks, int lane) {
  const int g = lane >> 4, q = (lane & 15) >> 2, p = lane & 3;
  const int r0 = 32 * ks + 8 * g + q, r1 = r0 + 4, ch = 2 * nt + (p >> 1);
  const s16x4 a = trread(lds + r0 * 256 + ((ch ^ swzB(r0)) << 4) + 8 * (p & 1));
  const s16x4 b = trread(lds + r1 * 256 + ((ch ^ swzB(r1)) << 4) + 8 * (p & 1));
  return cat8(a, b);
}
template <int MT, bool TR>
DEVI void mma_slab(f32x4 (&acc)[MT][4], unsigned la, unsigned lb, int mt0, int nta, int ntb, int lane) {
#pragma unroll
  for (int ks = 0; ks < 2; ++ks) {
    bf16x8 a[MT], b[4];
#pragma unroll
    for (int m = 0; m < MT; ++m) a[m] = fragA(la, mt0 + m, ks, lane);
    b[0] = fragB(lb, nta, ks, lane); b[1] = fragB(lb, nta + 1, ks, lane);
    b[2] = fragB(lb, ntb, ks, lane); b[3] = fragB(lb, ntb + 1, ks, lane);
#pragma unroll
    for (int m = 0; m < MT; ++m)
#pragma unroll
      for (int n = 0; n < 4; ++n) acc[m][n] = TR ? MFMA16(b[n], a[m], acc[m][n]) : MFMA16(a[m], b[n], acc[m][n]);
  }
}
DEVI u32x4 widen(unsigned a0, unsigned a1, unsigned b0, unsigned b1) {
  auto r0 = __builtin_amdgcn_permlane16_swap(a0, b0, false, false);
  auto r1 = __builtin_amdgcn_permlane16_swap(a1, b1, false, false);
  u32x4 o; o[0] = r0[0]; o[1] = r1[0]; o[2] = r0[1]; o[3] = r1[1]; return o;
}

template <int MODE, int BM>
__global__ __launch_bounds__(256, 2) void gemm_kernel(const u16* __restrict__ A, const u16* __restrict__ B0, const u16* __restrict__ B1,
                                                       u16* __restrict__ C0, u16* __restrict__ C1, const u16* __restrict__ R) {
  constexpr int MT = BM / 32;
  constexpr int ASZ = BM * 128, STG = ASZ + 16384;
  constexpr int LDA = MODE == 2 ? F_ : D_, NK = (MODE == 2 ? F_ : D_) / 64;
  constexpr int LDB = MODE == 0 ? HE : (MODE == 1 ? F_ : D_);
  constexpr int NMT = S_ / BM;
  __shared__ __attribute__((aligned(16))) char smem[2 * STG];
  const unsigned l0 = (unsigned)(uintptr_t)(lds_ptr)smem;
  const int t = threadIdx.x, lane = t & 63, w = __builtin_amdgcn_readfirstlane(t >> 6), wm = w >> 1, wn = w & 1;
  const int mi = blockIdx.x % NMT, ni = blockIdx.x / NMT;
  const int row0 = mi * BM;
  const u16 *b0, *b1; u16* C; int ccol;
  if (MODE == 0) { const bool isk = ni >= 32; const u16* Bm = isk ? B1 : B0; ccol = (ni & 31) * 128; b0 = Bm + ccol; b1 = b0 + 64; C = isk ? C1 : C0; }
  else if (MODE == 1) { ccol = ni * 64; b0 = B0 + ccol; b1 = B1 + ccol; C = C0; }
  else { ccol = ni * 128; b0 = B0 + ccol; b1 = b0 + 64; C = C0; }
  const int nta = MODE == 1 ? 2 * wn : 4 * wn, ntb = MODE == 1 ? 4 + 2 * wn : 4 * wn + 2;
  f32x4 acc[MT][4];
#pragma unroll
  for (int m = 0; m < MT; ++m)
#pragma unroll
    for (int n = 0; n < 4; ++n) acc[m][n] = f32x4{0.f, 0.f, 0.f, 0.f};
  stageA<BM>(A, LDA, row0, 0, l0, w, lane);
  stageB(b0, b1, LDB, 0, l0 + ASZ, w, lane);
  for (int kt = 0; kt < NK; ++kt) {
    BAR_ALL();
    const unsigned cur = l0 + (kt & 1) * STG, nxt = l0 + ((kt + 1) & 1) * STG;
    if (kt + 1 < NK) { stageA<BM>(A, LDA, row0, (kt + 1) * 64, nxt, w, lane); stageB(b0, b1, LDB, (kt + 1) * 64, nxt + ASZ, w, lane); }
    mma_slab<MT, true>(acc, cur, cur + ASZ, wm * MT, nta, ntb, lane);
  }
  const int g = lane >> 4, coff = 16 * (g & 1) + 8 * (g >> 1);
#pragma unroll
  for (int m = 0; m < MT; ++m) {
    const int row = row0 + (wm * MT + m) * 16 + (lane & 15);
    if (MODE == 0) {
#pragma unroll
      for (int pr = 0; pr < 2; ++pr) {
        const f32x4 x = acc[m][2 * pr], y = acc[m][2 * pr + 1];
        const u32x4 o = widen(cvtpk(x[0], x[1]), cvtpk(x[2], x[3]), cvtpk(y[0], y[1]), cvtpk(y[2], y[3]));
        *(u32x4*)(C + (size_t)row * HE + ccol + wn * 64 + pr * 32 + coff) = o;
      }
    } else if (MODE == 1) {
      float a[8];
#pragma unroll
      for (int i = 0; i < 8; ++i) {
        const float gt = rbf(acc[m][i >> 2][i & 3]), up = rbf(acc[m][2 + (i >> 2)][i & 3]);
        a[i] = (gt > 0.f ? gt : gt * 0.f) * up;
      }
      const u32x4 o = widen(cvtpk(a[0], a[1]), cvtpk(a[2], a[3]), cvtpk(a[4], a[5]), cvtpk(a[6], a[7]));
      *(u32x4*)(C + (size_t)row * F_ + ccol + wn * 32 + coff) = o;
    } else {
#pragma unroll
      for (int pr = 0; pr < 2; ++pr) {
        const f32x4 x = acc[m][2 * pr], y = acc[m][2 * pr + 1];
        const u32x4 o = widen(cvtpk(x[0], x[1]), cvtpk(x[2], x[3]), cvtpk(y[0], y[1]), cvtpk(y[2], y[3]));
        const size_t off = (size_t)row * D_ + ccol + wn * 64 + pr * 32 + coff;
        const u32x4 r = *(const u32x4*)(R + off);
        u32x4 z;
#pragma unroll
        for (int i = 0; i < 4; ++i) z[i] = cvtpk(bflo(r[i]) + bflo(o[i]), bfhi(r[i]) + bfhi(o[i]));
        *(u32x4*)(C + off) = z;
      }
    }
  }
}

__global__ __launch_bounds__(256, 2) void gemmVU_kernel(const u16* __restrict__ XN, const u16* __restrict__ Wv, const u16* __restrict__ Wo, char* __restrict__ U) {
  constexpr int STG = 32768;
  __shared__ __attribute__((aligned(16))) char smem[2 * STG];
  const unsigned l0 = (unsigned)(uintptr_t)(lds_ptr)smem;
  const int t = threadIdx.x, lane = t & 63, w = __builtin_amdgcn_readfirstlane(t >> 6), wm = w >> 1, wn = w & 1;
  const int mi = blockIdx.x & 15, h = blockIdx.x >> 4, row0 = mi * 128;
  const u16* b0 = Wv + h * 128;
  f32x4 acc[4][4];
#pragma unroll
  for (int m = 0; m < 4; ++m)
#pragma unroll
    for (int n = 0; n < 4; ++n) acc[m][n] = f32x4{0.f, 0.f, 0.f, 0.f};
  stageA<128>(XN, D_, row0, 0, l0, w, lane);
  stageB(b0, b0 + 64, HE, 0, l0 + 16384, w, lane);
  for (int kt = 0; kt < 10; ++kt) {
    BAR_ALL();
    const unsigned cur = l0 + (kt & 1) * STG, nxt = l0 + ((kt + 1) & 1) * STG;
    if (kt + 1 < 10) { stageA<128>(XN, D_, row0, (kt + 1) * 64, nxt, w, lane); stageB(b0, b0 + 64, HE, (kt + 1) * 64, nxt + 16384, w, lane); }
    mma_slab<4, true>(acc, cur, cur + 16384, wm * 4, 4 * wn, 4 * wn + 2, lane);
  }
  BAR_ALL();
  const u16* wo = Wo + (size_t)h * 128 * D_;
  stageB(wo, wo + 64, D_, 0, l0 + 16384, w, lane);
  stageB(wo, wo + 64, D_, 64, l0 + STG + 16384, w, lane);
  {
    const int g = lane >> 4;
#pragma unroll
    for (int m = 0; m < 4; ++m)
#pragma unroll
      for (int n = 0; n < 4; ++n) {
        const int row = (wm * 4 + m) * 16 + (lane & 15), ch = 2 * n + (g >> 1);
        u32x2 v; v[0] = cvtpk(acc[m][n][0], acc[m][n][1]); v[1] = cvtpk(acc[m][n][2], acc[m][n][3]);
        ldsw64(l0 + wn * STG + row * 128 + ((ch ^ (row & 7)) << 4) + 8 * (g & 1), v);
      }
  }
  for (int nb = 0; nb < 5; ++nb) {
    BAR_ALL();
#pragma unroll
    for (int m = 0; m < 4; ++m)
#pragma unroll
      for (int n = 0; n < 4; ++n) acc[m][n] = f32x4{0.f, 0.f, 0.f, 0.f};
    mma_slab<4, false>(acc, l0, l0 + 16384, wm * 4, 4 * wn, 4 * wn + 2, lane);
    mma_slab<4, false>(acc, l0 + STG, l0 + STG + 16384, wm * 4, 4 * wn, 4 * wn + 2, lane);
    BAR_LDS();
    if (nb + 1 < 5) {
      const u16* wn_ = wo + (nb + 1) * 128;
      stageB(wn_, wn_ + 64, D_, 0, l0 + 16384, w, lane);
      stageB(wn_, wn_ + 64, D_, 64, l0 + STG + 16384, w, lane);
    }
#pragma unroll
    for (int sl = 0; sl < 2; ++sl)
#pragma unroll
      for (int n = 0; n < 4; ++n) {
        const int seg = mi * 4 + wm * 2 + sl, ct = nb * 8 + wn * 4 + n;
        const f32x4 x = acc[2 * sl][n], y = acc[2 * sl + 1][n];
        u32x4 o; o[0] = cvtpk(x[0], x[1]); o[1] = cvtpk(x[2], x[3]); o[2] = cvtpk(y[0], y[1]); o[3] = cvtpk(y[2], y[3]);
        *(u32x4*)(U + ((size_t)(seg * 40 + ct) * 32 + h) * 1024 + lane * 16) = o;
      }
  }
}

constexpr float SCALE = 0.08838834764831845f, LOG2E = 1.4426950408889634f;
constexpr unsigned SPIN_BUDGET = 1u << 22;
constexpr int CH_STG = 65536;
constexpr int CH_MEM = 2 * CH_STG, CH_X = CH_MEM + 10240, CH_LDS = CH_X + 4096;

DEVI void poll_mem(const char* memp, unsigned lmem, int w, int lane, unsigned& budget) {
  const char* p0 = memp + w * 2560 + lane * 16;
  const char* p2 = memp + w * 2560 + 2048 + (lane & 31) * 16;
  u32x4 a0, a1, a2;
  for (;;) {
    asm volatile("global_load_dwordx4 %0, %3, off sc1\n\tglobal_load_dwordx4 %1, %3, off offset:1024 sc1\n\tglobal_load_dwordx4 %2, %4, off sc1\n\ts_waitcnt vmcnt(0)"
                 : "=&v"(a0), "=&v"(a1), "=&v"(a2) : "v"(p0), "v"(p2) : "memory");
    const unsigned m = max(max(umax4(a0), umax4(a1)), umax4(a2));
    if (__builtin_amdgcn_ballot_w64(m == ~0u) == 0 || budget == 0) break;
    --budget;
  }
  ldsw128(lmem + w * 2560 + lane * 16, a0);
  ldsw128(lmem + w * 2560 + 1024 + lane * 16, a1);
  ldsw128(lmem + w * 2560 + 2048 + (lane & 31) * 16, a2);
}

__global__ __launch_bounds__(256, 1) void chain_kernel(const u16* __restrict__ Wq, const u16* __restrict__ Wk, const u16* __restrict__ Wv,
                                                        const u16* __restrict__ Wo, char* ws) {
  __shared__ __attribute__((aligned(16))) char smem[CH_LDS];
  const unsigned l0 = (unsigned)(uintptr_t)(lds_ptr)smem;
  const int t = threadIdx.x, lane = t & 63, w = __builtin_amdgcn_readfirstlane(t >> 6);
  const int b = blockIdx.x;
  if (b >= 104) return;
  unsigned budget = SPIN_BUDGET;
  char* const memx = ws + OFF_MEM; char* const px = ws + OFF_P; char* const vwx = ws + OFF_VW;
  const int g = lane >> 4, q4 = (lane & 15) >> 2, p4 = lane & 3;

  if (b < 64) {
    const bool roleA = b < 32;
    const int h = b & 31;
    bf16x16 W[20][2];
    auto slab_src = [&](int sl, int row) -> const u16* {
      if (roleA) return (sl < 10 ? Wq : Wk) + (size_t)((sl % 10) * 64 + row) * HE + h * 128;
      if (sl < 10) return Wv + (size_t)(sl * 64 + row) * HE + h * 128;
      return Wo + (size_t)(h * 128 + ((sl - 10) & 1) * 64 + row) * D_ + ((sl - 10) >> 1) * 128;
    };
    auto stage_batch = [&](int bt) {
#pragma unroll
      for (int j = 0; j < 16; ++j) {
        const int sl = bt * 4 + (j >> 2), blk = w * 4 + (j & 3), pos = blk * 64 + lane;
        glds16(slab_src(sl, pos >> 4) + (pos & 15) * 8, l0 + (bt & 1) * CH_STG + (j >> 2) * 16384 + blk * 1024);
      }
    };
    stage_batch(0);
#pragma unroll
    for (int bt = 0; bt < 5; ++bt) {
      if (bt + 1 < 5) { stage_batch(bt + 1); asm volatile("s_waitcnt vmcnt(16)\n\ts_barrier" ::: "memory"); }
      else asm volatile("s_waitcnt vmcnt(0)\n\ts_barrier" ::: "memory");
#pragma unroll
      for (int j = 0; j < 4; ++j)
#pragma unroll
        for (int tt = 0; tt < 2; ++tt) {
          s16x4 r[4];
#pragma unroll
          for (int i = 0; i < 4; ++i) {
            const int row = 32 * (i >> 1) + 16 * (g >> 1) + 4 * (g & 1) + 2 * (i & 1) + (q4 & 1) + 8 * (q4 >> 1);
            r[i] = trread(l0 + (bt & 1) * CH_STG + j * 16384 + row * 256 + (32 * w + 16 * tt + 4 * p4) * 2);
          }
          const bf16x8 lo = cat8(r[0], r[1]), hi = cat8(r[2], r[3]);
          W[bt * 4 + j][tt] = __builtin_shufflevector(lo, hi, 0, 1, 2, 3, 4, 5, 6, 7, 8, 9, 10, 11, 12, 13, 14, 15);
        }
      BAR_LDS();
    }
    const int sidx = (lane & 8) ? 0xEEEE : 0x4444;
    const unsigned lmem = l0 + CH_MEM, lx = l0 + CH_X;

    if (roleA) {
      const u16* Kt = (const u16*)(ws + OFF_K) + h * 128 + (size_t)(lane & 15) * HE + 8 * g;
      u32x4 kf[2][4];
      if (w == 0) {
#pragma unroll
        for (int i = 0; i < 8; ++i) kf[i >> 2][i & 3] = *(const u32x4*)(Kt + (size_t)(16 * (i >> 2)) * HE + 32 * (i & 3));
      }
      for (int s = 0; s < NSEG; ++s) {
        poll_mem(memx + (size_t)s * MEM_STEP, lmem, w, lane, budget);
        BAR_LDS();
        f32x4 aq0 = {0.f, 0.f, 0.f, 0.f}, aq1 = aq0, ak0 = aq0, ak1 = aq0;
#pragma unroll
        for (int ks = 0; ks < 10; ++ks) {
          const bf16x8 a = asb8(ldsr128(lmem + ks * 1024 + lane * 16));
          aq0 = SMF(a, W[ks][0], aq0, sidx);
          aq1 = SMF(a, W[ks][1], aq1, sidx);
          ak0 = SMF(a, W[10 + ks][0], ak0, sidx);
          ak1 = SMF(a, W[10 + ks][1], ak1, sidx);
        }
        {
          u32x4 o;
          o[0] = cvtpk(pairsum32(ak0[0], ak1[0]), pairsum32(ak0[1], ak1[1]));
          o[1] = cvtpk(pairsum32(ak0[2], ak1[2]), pairsum32(ak0[3], ak1[3]));
          o[2] = cvtpk(pairsum32(aq0[0], aq1[0]), pairsum32(aq0[1], aq1[1]));
          o[3] = cvtpk(pairsum32(aq0[2], aq1[2]), pairsum32(aq0[3], aq1[3]));
          ldsw128(lx + (32 * w + 16 * (lane >> 5) + (lane & 15)) * 32 + (g & 1) * 16, o);
        }
        BAR_LDS();
        if (w == 0) {
          bf16x8 xf[4];
          const int pp = ((p4 & 1) << 1) | (p4 >> 1);
#pragma unroll
          for (int ks = 0; ks < 4; ++ks) {
            const s16x4 x0 = trread(lx + (32 * ks + 8 * g + q4) * 32 + pp * 8);
            const s16x4 x1 = trread(lx + (32 * ks + 8 * g + 4 + q4) * 32 + pp * 8);
            xf[ks] = cat8(x0, x1);
          }
          f32x4 s0 = {0.f, 0.f, 0.f, 0.f}, s1 = s0, s2 = s0;
#pragma unroll
          for (int ks = 0; ks < 4; ++ks) {
            s0 = MFMA16(xf[ks], xf[ks], s0);
            s1 = MFMA16(asb8(kf[0][ks]), xf[ks], s1);
            s2 = MFMA16(asb8(kf[1][ks]), xf[ks], s2);
          }
          float v[12];
#pragma unroll
          for (int i = 0; i < 4; ++i) {
            v[i] = g < 2 ? rbf(rbf(s0[i]) * SCALE) : -INFINITY;
            v[4 + i] = rbf(rbf(s1[i]) * SCALE);
            v[8 + i] = rbf(rbf(s2[i]) * SCALE);
          }
          float mx = v[0];
#pragma unroll
          for (int i = 1; i < 12; ++i) mx = fmaxf(mx, v[i]);
          mx = xmax32(xmax16(mx));
          float sum = 0.f;
#pragma unroll
          for (int i = 0; i < 12; ++i) { v[i] = __builtin_amdgcn_exp2f((v[i] - mx) * LOG2E); sum += v[i]; }
          sum = xsum32(xsum16(sum));
          const float inv = __builtin_amdgcn_rcpf(sum);
          u32x4 pt; u32x2 pm;
          pm[0] = nosent(cvtpk(v[0] * inv, v[1] * inv)); pm[1] = nosent(cvtpk(v[2] * inv, v[3] * inv));
          pt[0] = nosent(cvtpk(v[4] * inv, v[5] * inv)); pt[1] = nosent(cvtpk(v[6] * inv, v[7] * inv));
          pt[2] = nosent(cvtpk(v[8] * inv, v[9] * inv)); pt[3] = nosent(cvtpk(v[10] * inv, v[11] * inv));
          char* pd = px + (size_t)s * P_STEP + h * 640;
          if (lane & 8) {
            st_sc1_x4(pd + (lane & 7) * 64 + g * 16, pt);
            if (g < 2) st_sc1_x2(pd + 512 + (lane & 7) * 16 + g * 8, pm);
          }
          char* km = ws + OFF_KM + ((size_t)s * 32 + h) * 4096 + lane * 16;
#pragma unroll
          for (int ks = 0; ks < 4; ++ks) *(u32x4*)(km + ks * 1024) = __builtin_bit_cast(u32x4, xf[ks]);
          if (s + 1 < NSEG) {
#pragma unroll
            for (int i = 0; i < 8; ++i) kf[i >> 2][i & 3] = *(const u32x4*)(Kt + (size_t)((s + 1) * 32 + 16 * (i >> 2)) * HE + 32 * (i & 3));
          }
        }
      }
    } else {
      for (int s = 0; s < NSEG; ++s) {
        poll_mem(memx + (size_t)s * MEM_STEP, lmem, w, lane, budget);
        BAR_LDS();
        f32x4 a0 = {0.f, 0.f, 0.f, 0.f}, a1 = a0;
#pragma unroll
        for (int ks = 0; ks < 10; ++ks) {
          const bf16x8 a = asb8(ldsr128(lmem + ks * 1024 + lane * 16));
          a0 = SMF(a, W[ks][0], a0, sidx);
          a1 = SMF(a, W[ks][1], a1, sidx);
        }
        {
          u32x2 o;
          o[0] = cvtpk(pairsum32(a0[0], a1[0]), pairsum32(a0[1], a1[1]));
          o[1] = cvtpk(pairsum32(a0[2], a1[2]), pairsum32(a0[3], a1[3]));
          ldsw64(lx + (32 * w + 16 * (lane >> 5) + (lane & 15)) * 16 + (g & 1) * 8, o);
        }
        BAR_LDS();
        bf16x8 va[2];
#pragma unroll
        for (int ks = 0; ks < 2; ++ks) {
          const s16x4 x0 = trread(lx + (64 * ks + 16 * g + 8 * (p4 >> 1) + q4) * 16 + (p4 & 1) * 8);
          const s16x4 x1 = trread(lx + (64 * ks + 16 * g + 8 * (p4 >> 1) + 4 + q4) * 16 + (p4 & 1) * 8);
          va[ks] = cat8(x0, x1);
        }
        char* vd = vwx + (size_t)s * VW_STEP + (size_t)(2 * w + (lane >> 5)) * 8192 + h * 256 + (lane & 15) * 16 + (g & 1) * 8;
#pragma unroll
        for (int cb = 0; cb < 5; ++cb) {
          f32x4 c0 = {0.f, 0.f, 0.f, 0.f}, c1 = c0;
          c0 = SMF(va[0], W[10 + 2 * cb][0], c0, sidx);
          c1 = SMF(va[0], W[10 + 2 * cb][1], c1, sidx);
          c0 = SMF(va[1], W[11 + 2 * cb][0], c0, sidx);
          c1 = SMF(va[1], W[11 + 2 * cb][1], c1, sidx);
          u32x2 o;
          o[0] = nosent(cvtpk(pairsum32(c0[0], c1[0]), pairsum32(c0[1], c1[1])));
          o[1] = nosent(cvtpk(pairsum32(c0[2], c1[2]), pairsum32(c0[3], c1[3])));
          st_sc1_x2(vd + cb * 65536, o);
        }
      }
    }
  } else {
    const int ct = b - 64;
    const char* up = ws + OFF_U + ((size_t)ct * 32 + 8 * w) * 1024 + lane * 16;
    u32x4 uf[8];
#pragma unroll
    for (int i = 0; i < 8; ++i) uf[i] = *(const u32x4*)(up + i * 1024);
    const int r = lane & 7, hb = (lane >> 3) & 1;
    const unsigned lred = l0;
    for (int s = 0; s < NSEG - 1; ++s) {
      const char* pt = px + (size_t)s * P_STEP + (8 * w + hb) * 640 + r * 64 + g * 16;
      const char* pm = px + (size_t)s * P_STEP + (8 * w + 4 * hb + g) * 640 + 512 + r * 16;
      const char* vp = vwx + (size_t)s * VW_STEP + (size_t)ct * 8192 + 8 * w * 256 + lane * 16;
      u32x4 t0, t1, t2, t3, m0, v0, v1;
      for (;;) {
        asm volatile("global_load_dwordx4 %5, %9, off sc1\n\tglobal_load_dwordx4 %6, %9, off offset:1024 sc1\n\t"
                     "global_load_dwordx4 %0, %7, off sc1\n\tglobal_load_dwordx4 %1, %7, off offset:1280 sc1\n\t"
                     "global_load_dwordx4 %2, %7, off offset:2560 sc1\n\tglobal_load_dwordx4 %3, %7, off offset:3840 sc1\n\t"
                     "global_load_dwordx4 %4, %8, off sc1\n\ts_waitcnt vmcnt(0)"
                     : "=&v"(t0), "=&v"(t1), "=&v"(t2), "=&v"(t3), "=&v"(m0), "=&v"(v0), "=&v"(v1) : "v"(pt), "v"(pm), "v"(vp) : "memory");
        const unsigned m = max(max(max(umax4(t0), umax4(t1)), max(umax4(t2), umax4(t3))), max(umax4(m0), max(umax4(v0), umax4(v1))));
        if (__builtin_amdgcn_ballot_w64(m == ~0u) == 0 || budget == 0) break;
        --budget;
      }
      f32x4 ae = {0.f, 0.f, 0.f, 0.f}, ao = ae;
      ae = MFMA16(asb8(uf[0]), asb8(t0), ae); ao = MFMA16(asb8(uf[1]), asb8(t0), ao);
      ae = MFMA16(asb8(uf[2]), asb8(t1), ae); ao = MFMA16(asb8(uf[3]), asb8(t1), ao);
      ae = MFMA16(asb8(uf[4]), asb8(t2), ae); ao = MFMA16(asb8(uf[5]), asb8(t2), ao);
      ae = MFMA16(asb8(uf[6]), asb8(t3), ae); ao = MFMA16(asb8(uf[7]), asb8(t3), ao);
      ae = MFMA16(asb8(v0), asb8(m0), ae);    ao = MFMA16(asb8(v1), asb8(m0), ao);
      const f32x4 sel = hb ? ao : ae;
      ldsw128(lred + ((w * 2 + hb) * 8 + r) * 64 + g * 16, __builtin_bit_cast(u32x4, sel));
      BAR_LDS();
      if (w == 0) {
        const int cq = lane >> 3;
        float x0 = 0.f, x1 = 0.f;
#pragma unroll
        for (int i = 0; i < 8; ++i) { const u32x2 z = ldsr64(lred + (i * 8 + r) * 64 + cq * 8); x0 += __uint_as_float(z[0]); x1 += __uint_as_float(z[1]); }
        st_sc1_x1(memx + (size_t)(s + 1) * MEM_STEP + ct * 256 + (cq >> 2) * 128 + r * 16 + (cq & 3) * 4, nosent(cvtpk(x0, x1)));
      }
      if (s + 2 < NSEG) {
#pragma unroll
        for (int i = 0; i < 8; ++i) uf[i] = *(const u32x4*)(up + (size_t)(s + 1) * (40 * 32 * 1024) + i * 1024);
      }
    }
  }
}

__global__ __launch_bounds__(256) void tokP_kernel(char* __restrict__ ws) {
  const int t = threadIdx.x, lane = t & 63, w = t >> 6;
  const int seg = blockIdx.x >> 3, h = (blockIdx.x & 7) * 4 + w;
  const int g = lane >> 4, c = lane & 15;
  const u16* Q = (const u16*)(ws + OFF_Q) + (size_t)(seg * 32 + c) * HE + h * 128 + 8 * g;
  const u16* K = (const u16*)(ws + OFF_K) + (size_t)(seg * 32 + c) * HE + h * 128 + 8 * g;
  const char* km = ws + OFF_KM + ((size_t)seg * 32 + h) * 4096 + lane * 16;
  f32x4 s[3][2];
#pragma unroll
  for (int i = 0; i < 6; ++i) s[i >> 1][i & 1] = f32x4{0.f, 0.f, 0.f, 0.f};
#pragma unroll
  for (int ks = 0; ks < 4; ++ks) {
    const bf16x8 m = asb8(*(const u32x4*)(km + ks * 1024));
    const bf16x8 k0 = asb8(*(const u32x4*)(K + 32 * ks)), k1 = asb8(*(const u32x4*)(K + (size_t)16 * HE + 32 * ks));
    const bf16x8 q0 = asb8(*(const u32x4*)(Q + 32 * ks)), q1 = asb8(*(const u32x4*)(Q + (size_t)16 * HE + 32 * ks));
    s[0][0] = MFMA16(m, q0, s[0][0]); s[0][1] = MFMA16(m, q1, s[0][1]);
    s[1][0] = MFMA16(k0, q0, s[1][0]); s[1][1] = MFMA16(k0, q1, s[1][1]);
    s[2][1] = MFMA16(k1, q1, s[2][1]);
  }
  char* pd = ws + OFF_PT + ((size_t)seg * 32 + h) * 3072;
#pragma unroll
  for (int qt = 0; qt < 2; ++qt) {
    float v[12];
#pragma unroll
    for (int i = 0; i < 4; ++i) {
      v[i] = g < 2 ? rbf(rbf(s[0][qt][i]) * SCALE) : -INFINITY;
      v[4 + i] = (4 * g + i <= 16 * qt + c) ? rbf(rbf(s[1][qt][i]) * SCALE) : -INFINITY;
      v[8 + i] = (16 + 4 * g + i <= 16 * qt + c) ? rbf(rbf(s[2][qt][i]) * SCALE) : -INFINITY;
    }
    float mx = v[0];
#pragma unroll
    for (int i = 1; i < 12; ++i) mx = fmaxf(mx, v[i]);
    mx = xmax32(xmax16(mx));
    float sum = 0.f;
#pragma unroll
    for (int i = 0; i < 12; ++i) { v[i] = __builtin_amdgcn_exp2f((v[i] - mx) * LOG2E); sum += v[i]; }
    sum = xsum32(xsum16(sum));
    const float inv = 1.0f / sum;
    u32x4 pt; u32x2 pm;
    pm[0] = cvtpk(v[0] * inv, v[1] * inv); pm[1] = cvtpk(v[2] * inv, v[3] * inv);
    pt[0] = cvtpk(v[4] * inv, v[5] * inv); pt[1] = cvtpk(v[6] * inv, v[7] * inv);
    pt[2] = cvtpk(v[8] * inv, v[9] * inv); pt[3] = cvtpk(v[10] * inv, v[11] * inv);
    *(u32x4*)(pd + qt * 1024 + lane * 16) = pt;
    *(u32x2*)(pd + 2048 + qt * 512 + lane * 8) = pm;
  }
}

__global__ __launch_bounds__(256) void tokO_kernel(const u16* __restrict__ x, char* __restrict__ ws) {
  const int t = threadIdx.x, lane = t & 63, w = t >> 6;
  const int seg = blockIdx.x / 5, ct0 = (blockIdx.x % 5) * 8 + 2 * w;
  const int g = lane >> 4, c = lane & 15;
  const char* up = ws + OFF_U + (size_t)(seg * 40 + ct0) * 32768 + lane * 16;
  const char* pp = ws + OFF_PT + (size_t)seg * 32 * 3072;
  const char* vp = ws + OFF_VW + (size_t)seg * VW_STEP + (size_t)ct0 * 8192 + c * 16 + (g & 1) * 8;
  f32x4 acc[2][2];
#pragma unroll
  for (int i = 0; i < 4; ++i) acc[i >> 1][i & 1] = f32x4{0.f, 0.f, 0.f, 0.f};
#pragma unroll 4
  for (int hp = 0; hp < 16; ++hp) {
    u32x4 u[2][2], p[2][2]; u32x2 pm[2][2], vw[2][2];
#pragma unroll
    for (int hh = 0; hh < 2; ++hh) {
      const int h = 2 * hp + hh;
#pragma unroll
      for (int i = 0; i < 2; ++i) {
        u[hh][i] = *(const u32x4*)(up + i * 32768 + h * 1024);
        p[hh][i] = *(const u32x4*)(pp + h * 3072 + i * 1024 + lane * 16);
        pm[hh][i] = *(const u32x2*)(pp + h * 3072 + 2048 + i * 512 + lane * 8);
        vw[hh][i] = *(const u32x2*)(vp + i * 8192 + h * 256);
      }
    }
#pragma unroll
    for (int ci = 0; ci < 2; ++ci)
#pragma unroll
      for (int qt = 0; qt < 2; ++qt) {
        acc[ci][qt] = MFMA16(asb8(u[0][ci]), asb8(p[0][qt]), acc[ci][qt]);
        acc[ci][qt] = MFMA16(asb8(u[1][ci]), asb8(p[1][qt]), acc[ci][qt]);
        u32x4 a, bb;
        a[0] = vw[0][ci][0]; a[1] = vw[0][ci][1]; a[2] = vw[1][ci][0]; a[3] = vw[1][ci][1];
        bb[0] = pm[0][qt][0]; bb[1] = pm[0][qt][1]; bb[2] = pm[1][qt][0]; bb[3] = pm[1][qt][1];
        acc[ci][qt] = MFMA16(asb8(a), asb8(bb), acc[ci][qt]);
      }
  }
  u16* H = (u16*)(ws + OFF_H);
#pragma unroll
  for (int qt = 0; qt < 2; ++qt) {
    const f32x4 a = acc[0][qt], bq = acc[1][qt];
    const u32x4 o = widen(cvtpk(a[0], a[1]), cvtpk(a[2], a[3]), cvtpk(bq[0], bq[1]), cvtpk(bq[2], bq[3]));
    const size_t off = (size_t)(seg * 32 + qt * 16 + c) * D_ + ct0 * 16 + 16 * (g & 1) + 8 * (g >> 1);
    const u32x4 r = *(const u32x4*)(x + off);
    u32x4 z;
#pragma unroll
    for (int i = 0; i < 4; ++i) z[i] = cvtpk(bflo(r[i]) + bflo(o[i]), bfhi(r[i]) + bfhi(o[i]));
    *(u32x4*)(H + off) = z;
  }
}

extern "C" void kernel_launch(void* const* d_in, const int* in_sizes, int n_in, void* d_out, int out_size, void* d_ws, size_t ws_size,
                              hipStream_t stream) {
  const u16* x = (const u16*)d_in[0];
  const u16* mem0 = (const u16*)d_in[1];
  const u16* Wq = (const u16*)d_in[2];
  const u16* Wk = (const u16*)d_in[3];
  const u16* Wv = (const u16*)d_in[4];
  const u16* Wo = (const u16*)d_in[5];
  const u16* Wg = (const u16*)d_in[8];
  const u16* Wu = (const u16*)d_in[9];
  const u16* Wd = (const u16*)d_in[10];
  char* ws = (char*)d_ws;
  u16* XN = (u16*)(ws + OFF_XN);
  prep_kernel<<<512 + FILL_BLOCKS + 1, 256, 0, stream>>>((const unsigned*)x, mem0, ws);
  gemm_kernel<0, 128><<<16 * 64, 256, 0, stream>>>(XN, Wq, Wk, (u16*)(ws + OFF_Q), (u16*)(ws + OFF_K), XN);
  gemmVU_kernel<<<16 * 32, 256, 0, stream>>>(XN, Wv, Wo, ws + OFF_U);
  chain_kernel<<<104, 256, 0, stream>>>(Wq, Wk, Wv, Wo, ws);
  tokP_kernel<<<64 * 8, 256, 0, stream>>>(ws);
  tokO_kernel<<<64 * 5, 256, 0, stream>>>(x, ws);
  ln2_kernel<<<512, 256, 0, stream>>>(ws);
  gemm_kernel<1, 128><<<16 * 27, 256, 0, stream>>>((const u16*)(ws + OFF_HN), Wg, Wu, (u16*)(ws + OFF_ACT), (u16*)(ws + OFF_ACT), XN);
  gemm_kernel<2, 64><<<32 * 5, 256, 0, stream>>>((const u16*)(ws + OFF_ACT), Wd, Wd, (u16*)d_out, (u16*)d_out, (const u16*)(ws + OFF_H));
}
```

```cpp
#include <hip/hip_runtime.h>
#include <hip/hip_bf16.h>
#include <stdint.h>

typedef __bf16 bf16x8 __attribute__((ext_vector_type(8)));
typedef __bf16 bf16x16 __attribute__((ext_vector_type(16)));
typedef __bf16 bf16x4 __attribute__((ext_vector_type(4)));
typedef float f32x4 __attribute__((ext_vector_type(4)));
typedef unsigned u32x4 __attribute__((ext_vector_type(4)));
typedef unsigned u32x2 __attribute__((ext_vector_type(2)));
typedef short s16x4 __attribute__((ext_vector_type(4)));
typedef __attribute__((address_space(3))) char* lds_ptr;
typedef unsigned short u16;

#define DEVI __device__ __forceinline__

constexpr int S_ = 2048, D_ = 640, HE = 4096, F_ = 1728, NSEG = 64;
constexpr int NPRE = 8;
constexpr int NFILL = 60;
constexpr unsigned XN_SEG = 40960, QK_SEG = 32 * 16384, U_SEG = 40 * 32 * 1024;
constexpr unsigned OFF_XN = 0;
constexpr unsigned OFF_QK = OFF_XN + NSEG * XN_SEG;
constexpr unsigned OFF_U = OFF_QK + NSEG * QK_SEG;
constexpr unsigned OFF_FLG = OFF_U + NSEG * U_SEG;
constexpr unsigned OFF_R = OFF_FLG + 72 * 256;
constexpr unsigned R_MEM = 0, R_P = 10240, R_VW = R_P + 20480, R_PT = R_VW + 327680, R_H = R_PT + 32 * 2560, R_ACT = R_H + 40960,
                   REC = R_ACT + 54 * 2048;
constexpr unsigned OFF_END = OFF_R + NSEG * REC;
static_assert(OFF_R % 256 == 0 && REC % 256 == 0 && OFF_U % 256 == 0, "align");
static_assert((size_t)OFF_END + (4u << 20) <= (size_t)256 * 1024 * 1024, "ws");

typedef __bf16 bf16x2 __attribute__((ext_vector_type(2)));
typedef float f32x2 __attribute__((ext_vector_type(2)));
DEVI unsigned cvtpk(float lo, float hi) { const f32x2 v = {lo, hi}; return __builtin_bit_cast(unsigned, __builtin_convertvector(v, bf16x2)); }
DEVI float bflo(unsigned u) { return __uint_as_float(u << 16); }
DEVI float bfhi(unsigned u) { return __uint_as_float(u & 0xffff0000u); }
DEVI float rbf(float x) { return bflo(cvtpk(x, x)); }
DEVI unsigned nosent(unsigned u) { return u < 0xFFFFFFFEu ? u : 0xFFFFFFFEu; }
DEVI void glds16(const void* g, unsigned lds_base) {
  unsigned sv;
  asm volatile("s_mov_b32 %0, m0\n\ts_mov_b32 m0, %2\n\ts_nop 0\n\tglobal_load_lds_dwordx4 %1, off\n\ts_mov_b32 m0, %0" : "=&s"(sv) : "v"(g), "s"(lds_base) : "memory");
}
DEVI s16x4 trread(unsigned a) { return __builtin_amdgcn_ds_read_tr16_b64_v4i16((__attribute__((address_space(3))) s16x4*)(uintptr_t)a); }
DEVI bf16x8 cat8(s16x4 a, s16x4 b) { return __builtin_bit_cast(bf16x8, __builtin_shufflevector(a, b, 0, 1, 2, 3, 4, 5, 6, 7)); }
DEVI u32x4 ldsr128(unsigned a) { return *(__attribute__((address_space(3))) u32x4*)(uintptr_t)a; }
DEVI u32x2 ldsr64(unsigned a) { return *(__attribute__((address_space(3))) u32x2*)(uintptr_t)a; }
DEVI void ldsw128(unsigned a, u32x4 v) { *(__attribute__((address_space(3))) u32x4*)(uintptr_t)a = v; }
DEVI void ldsw64(unsigned a, u32x2 v) { *(__attribute__((address_space(3))) u32x2*)(uintptr_t)a = v; }
DEVI bf16x8 asb8(u32x4 v) { return __builtin_bit_cast(bf16x8, v); }
#define MFMA16(a, b, c) __builtin_amdgcn_mfma_f32_16x16x32_bf16(a, b, c, 0, 0, 0)
#define SMF(a, b, c, idx) __builtin_amdgcn_smfmac_f32_16x16x64_bf16(a, b, c, idx, 0, 0)
#define BAR_ALL() asm volatile("s_waitcnt vmcnt(0) lgkmcnt(0)\n\ts_barrier" ::: "memory")
#define BAR_LDS() asm volatile("s_waitcnt lgkmcnt(0)\n\ts_barrier" ::: "memory")
DEVI float xmax16(float x) { auto r = __builtin_amdgcn_permlane16_swap(__float_as_uint(x), __float_as_uint(x), false, false); return fmaxf(__uint_as_float(r[0]), __uint_as_float(r[1])); }
DEVI float xmax32(float x) { auto r = __builtin_amdgcn_permlane32_swap(__float_as_uint(x), __float_as_uint(x), false, false); return fmaxf(__uint_as_float(r[0]), __uint_as_float(r[1])); }
DEVI float xsum16(float x) { auto r = __builtin_amdgcn_permlane16_swap(__float_as_uint(x), __float_as_uint(x), false, false); return __uint_as_float(r[0]) + __uint_as_float(r[1]); }
DEVI float xsum32(float x) { auto r = __builtin_amdgcn_permlane32_swap(__float_as_uint(x), __float_as_uint(x), false, false); return __uint_as_float(r[0]) + __uint_as_float(r[1]); }
DEVI float pairsum32(float a, float b) { auto r = __builtin_amdgcn_permlane32_swap(__float_as_uint(a), __float_as_uint(b), false, false); return __uint_as_float(r[0]) + __uint_as_float(r[1]); }
DEVI void st_sc1_x4(void* p, u32x4 v) { asm volatile("global_store_dwordx4 %0, %1, off sc1\n\ts_nop 1" ::"v"(p), "v"(v) : "memory"); }
DEVI void st_sc1_x2(void* p, u32x2 v) { asm volatile("global_store_dwordx2 %0, %1, off sc1\n\ts_nop 1" ::"v"(p), "v"(v) : "memory"); }
DEVI void st_sc1_x1(void* p, unsigned v) { asm volatile("global_store_dword %0, %1, off sc1\n\ts_nop 1" ::"v"(p), "v"(v) : "memory"); }
DEVI unsigned umax4(u32x4 v) { return max(max(v[0], v[1]), max(v[2], v[3])); }

typedef unsigned long long u64;
typedef __amdgpu_buffer_rsrc_t rsrc_t;
DEVI u32x4 ld16(rsrc_t rs, unsigned v, unsigned s) { return __builtin_amdgcn_raw_buffer_load_b128(rs, v, s, 16); }
DEVI unsigned ld4(rsrc_t rs, unsigned v, unsigned s) { return __builtin_amdgcn_raw_buffer_load_b32(rs, v, s, 16); }
DEVI void st16(rsrc_t rs, unsigned v, unsigned s, u32x4 d) { __builtin_amdgcn_raw_buffer_store_b128(d, rs, v, s, 16); }
DEVI void st8(rsrc_t rs, unsigned v, unsigned s, u32x2 d) { __builtin_amdgcn_raw_buffer_store_b64(d, rs, v, s, 16); }
DEVI void st4(rsrc_t rs, unsigned v, unsigned s, unsigned d) { __builtin_amdgcn_raw_buffer_store_b32(d, rs, v, s, 16); }
#define NOMOVE() asm volatile("" ::: "memory")
#define PENDING(v) __builtin_amdgcn_ballot_w64(umax4(v) == ~0u)
DEVI float xsum1(float x) { return x + __uint_as_float(__builtin_amdgcn_update_dpp(0u, __float_as_uint(x), 0xB1, 0xF, 0xF, true)); }
DEVI u32x4 widen(unsigned a0, unsigned a1, unsigned b0, unsigned b1) {
  auto r0 = __builtin_amdgcn_permlane16_swap(a0, b0, false, false);
  auto r1 = __builtin_amdgcn_permlane16_swap(a1, b1, false, false);
  u32x4 o; o[0] = r0[0]; o[1] = r1[0]; o[2] = r0[1]; o[3] = r1[1]; return o;
}
DEVI u32x4 widen4(f32x4 x, f32x4 y) { return widen(cvtpk(x[0], x[1]), cvtpk(x[2], x[3]), cvtpk(y[0], y[1]), cvtpk(y[2], y[3])); }
DEVI u32x4 nosent4(u32x4 v) { u32x4 o; o[0] = nosent(v[0]); o[1] = nosent(v[1]); o[2] = nosent(v[2]); o[3] = nosent(v[3]); return o; }

template <int N, class RED>
DEVI void ln_apply(u32x4 (&v)[N], RED red) {
  float s = 0.f;
#pragma unroll
  for (int i = 0; i < N; ++i)
#pragma unroll
    for (int j = 0; j < 4; ++j) s += bflo(v[i][j]) + bfhi(v[i][j]);
  const float mean = red(s) * (1.0f / 640.0f);
  float q = 0.f;
#pragma unroll
  for (int i = 0; i < N; ++i)
#pragma unroll
    for (int j = 0; j < 4; ++j) { const float a = bflo(v[i][j]) - mean, b = bfhi(v[i][j]) - mean; q += a * a + b * b; }
  const float mb = rbf(mean), vb = rbf(red(q) * (1.0f / 640.0f));
  const float sd = rbf(sqrtf(rbf(vb + 1e-05f)));
#pragma unroll
  for (int i = 0; i < N; ++i)
#pragma unroll
    for (int j = 0; j < 4; ++j) v[i][j] = cvtpk(rbf(bflo(v[i][j]) - mb) / sd, rbf(bfhi(v[i][j]) - mb) / sd);
}

template <int N, class RED>
DEVI void ln_fast(u32x4 (&v)[N], RED red) {
  f32x2 s2 = {0.f, 0.f};
#pragma unroll
  for (int i = 0; i < N; ++i)
#pragma unroll
    for (int j = 0; j < 4; ++j) s2 += f32x2{bflo(v[i][j]), bfhi(v[i][j])};
  const float mean = red(s2[0] + s2[1]) * (1.0f / 640.0f);
  const float mb = rbf(mean);
  const f32x2 m2 = {mb, mb};
  f32x2 q2 = {0.f, 0.f};
#pragma unroll
  for (int i = 0; i < N; ++i)
#pragma unroll
    for (int j = 0; j < 4; ++j) { const f32x2 d = f32x2{bflo(v[i][j]), bfhi(v[i][j])} - m2; q2 += d * d; v[i][j] = cvtpk(d[0], d[1]); }
  const float dm = mean - mb;
  const float vb = rbf(fmaxf(red(q2[0] + q2[1]) * (1.0f / 640.0f) - dm * dm, 0.f));
  const float r = 1.0f / rbf(sqrtf(rbf(vb + 1e-05f)));
  const f32x2 r2 = {r, r};
#pragma unroll
  for (int i = 0; i < N; ++i)
#pragma unroll
    for (int j = 0; j < 4; ++j) { const f32x2 d = f32x2{bflo(v[i][j]), bfhi(v[i][j])} * r2; v[i][j] = cvtpk(d[0], d[1]); }
}

constexpr int FILL_BLOCKS = 256;
__global__ __launch_bounds__(256) void prep_kernel(const u16* __restrict__ x, const u16* __restrict__ mem0, char* __restrict__ ws) {
  const int b = blockIdx.x, t = threadIdx.x;
  if (b < 128) {
    const int row = b * 16 + (t >> 4), i = t & 15;
    u32x4 v[5];
#pragma unroll
    for (int j = 0; j < 5; ++j) v[j] = *(const u32x4*)(x + (size_t)row * D_ + (i + 16 * j) * 8);
    ln_apply<5>(v, [](float s) { s += __shfl_xor(s, 1); s += __shfl_xor(s, 2); s += __shfl_xor(s, 4); s += __shfl_xor(s, 8); return s; });
    char* dst = ws + OFF_XN + (size_t)(row >> 5) * XN_SEG + ((row >> 4) & 1) * 1024 + ((i & 3) * 16 + (row & 15)) * 16;
#pragma unroll
    for (int j = 0; j < 5; ++j) *(u32x4*)(dst + (4 * j + (i >> 2)) * 2048) = v[j];
  } else if (b < 128 + FILL_BLOCKS) {
    u32x4* p = (u32x4*)(ws + OFF_R + R_P);
    const unsigned n = (NPRE * REC - R_P) / 16;
    const u32x4 s = {~0u, ~0u, ~0u, ~0u};
    for (unsigned i = (b - 128) * 256 + t; i < n; i += FILL_BLOCKS * 256) p[i] = s;
  } else {
    if (t < 72) ((unsigned*)(ws + OFF_FLG))[t * 64] = 0u;
    for (int i = t; i < 640; i += 256) {
      const int ct = i >> 4, bb = (i >> 3) & 1, r = i & 7;
      *(u32x4*)(ws + OFF_R + R_MEM + (size_t)i * 16) = nosent4(*(const u32x4*)(mem0 + r * 640 + ct * 16 + bb * 8));
    }
  }
}

constexpr float SCALE = 0.08838834764831845f, LOG2E = 1.4426950408889634f;
constexpr unsigned SPIN_BUDGET = 1u << 22;
constexpr int CH_STG = 65536;
constexpr int CH_MEM = 2 * CH_STG, CH_X = CH_MEM + 10240, CH_LDS = CH_X + 4096;

#define TS(i)
#define TSW(i)
#define TSDECL
#define TSFLUSH(s)
DEVI void poll_mem(const char* memp, unsigned lmem, int w, int lane, unsigned& budget) {
  const char* p0 = memp + w * 2560 + lane * 16;
  u32x4 a0 = {~0u, ~0u, ~0u, ~0u}, a1 = a0, a2 = {0u, 0u, 0u, 0u};
  u64 m0 = ~0ull, m1 = ~0ull, m2 = 0xFFFFFFFFull, sv;
  for (;;) {
    asm volatile("s_mov_b64 %[sv], exec\n\t"
                 "s_mov_b64 exec, %[m0]\n\tglobal_load_dwordx4 %[a0], %[p0], off sc1\n\t"
                 "s_mov_b64 exec, %[m1]\n\tglobal_load_dwordx4 %[a1], %[p0], off offset:1024 sc1\n\t"
                 "s_mov_b64 exec, %[m2]\n\tglobal_load_dwordx4 %[a2], %[p0], off offset:2048 sc1\n\t"
                 "s_mov_b64 exec, %[sv]\n\ts_waitcnt vmcnt(0)"
                 : [a0] "+v"(a0), [a1] "+v"(a1), [a2] "+v"(a2), [sv] "=&s"(sv)
                 : [m0] "s"(m0), [m1] "s"(m1), [m2] "s"(m2), [p0] "v"(p0) : "memory");
    m0 = PENDING(a0); m1 = PENDING(a1); m2 = PENDING(a2);
    if ((m0 | m1 | m2) == 0 || budget == 0) break;
    --budget;
  }
  ldsw128(lmem + w * 2560 + lane * 16, a0);
  ldsw128(lmem + w * 2560 + 1024 + lane * 16, a1);
  if (lane < 32) ldsw128(lmem + w * 2560 + 2048 + lane * 16, a2);
}
DEVI void wait_prog(rsrc_t rs, unsigned voff, unsigned need, unsigned& seen, unsigned& budget) {
  while (__builtin_amdgcn_ballot_w64(seen < need) != 0 && budget != 0) {
    NOMOVE();
    seen = ld4(rs, voff, OFF_FLG);
    --budget;
    if (__builtin_amdgcn_ballot_w64(seen < need) != 0) __builtin_amdgcn_s_sleep(8);
  }
  NOMOVE();
}
DEVI void wait_fill(rsrc_t rs, int s, unsigned& budget) {
  if ((s & (NPRE - 1)) == NPRE - 3 && s + NPRE < NSEG) { unsigned seen = 0; wait_prog(rs, (64 + s / NPRE) * 256, NFILL, seen, budget); }
}
DEVI void wait_word(rsrc_t rs, unsigned soff, unsigned& budget) {
  for (;;) {
    NOMOVE();
    const unsigned v = ld4(rs, 0, soff);
    if (__builtin_amdgcn_readfirstlane(v) != ~0u || budget == 0) break;
    --budget;
    __builtin_amdgcn_s_sleep(2);
  }
  NOMOVE();
}
template <int N, class OFFV, class OFFS>
DEVI void poll_n(rsrc_t rs, u32x4 (&v)[N], OFFV offv, OFFS offs, unsigned& budget, unsigned pend = (1u << N) - 1u) {
  for (;;) {
    NOMOVE();
#pragma unroll
    for (int i = 0; i < N; ++i) if (pend & (1u << i)) v[i] = ld16(rs, offv(i), offs(i));
    unsigned np = 0;
#pragma unroll
    for (int i = 0; i < N; ++i) np |= (PENDING(v[i]) != 0 ? 1u : 0u) << i;
    pend = np;
    if (pend == 0 || budget == 0) break;
    --budget;
  }
  NOMOVE();
}
template <bool AG>
DEVI void smf2(f32x4& c0, f32x4& c1, bf16x8 a, const bf16x16& w0, const bf16x16& w1, int idx) {
  if (AG) asm volatile("v_smfmac_f32_16x16x64_bf16 %0, %2, %3, %5\n\tv_smfmac_f32_16x16x64_bf16 %1, %2, %4, %5" : "+v"(c0), "+v"(c1) : "v"(a), "a"(w0), "a"(w1), "v"(idx));
  else asm volatile("v_smfmac_f32_16x16x64_bf16 %0, %2, %3, %5\n\tv_smfmac_f32_16x16x64_bf16 %1, %2, %4, %5" : "+v"(c0), "+v"(c1) : "v"(a), "v"(w0), "v"(w1), "v"(idx));
}
template <bool AG, bool WA, bool FIRST>
DEVI void mf2(f32x4& c0, f32x4& c1, const bf16x8& w0, const bf16x8& w1, bf16x8 x) {
#define MF2_ASM(T, CO, WC) asm volatile(T : CO(c0), CO(c1) : WC(w0), WC(w1), "v"(x))
#define T_WA_F "v_mfma_f32_16x16x32_bf16 %0, %2, %4, 0\n\tv_mfma_f32_16x16x32_bf16 %1, %3, %4, 0"
#define T_WA_N "v_mfma_f32_16x16x32_bf16 %0, %2, %4, %0\n\tv_mfma_f32_16x16x32_bf16 %1, %3, %4, %1"
#define T_WB_F "v_mfma_f32_16x16x32_bf16 %0, %4, %2, 0\n\tv_mfma_f32_16x16x32_bf16 %1, %4, %3, 0"
#define T_WB_N "v_mfma_f32_16x16x32_bf16 %0, %4, %2, %0\n\tv_mfma_f32_16x16x32_bf16 %1, %4, %3, %1"
  if (AG) {
    if (WA) { if (FIRST) MF2_ASM(T_WA_F, "=&v", "a"); else MF2_ASM(T_WA_N, "+v", "a"); }
    else { if (FIRST) MF2_ASM(T_WB_F, "=&v", "a"); else MF2_ASM(T_WB_N, "+v", "a"); }
  } else {
    if (WA) { if (FIRST) MF2_ASM(T_WA_F, "=&v", "v"); else MF2_ASM(T_WA_N, "+v", "v"); }
    else { if (FIRST) MF2_ASM(T_WB_F, "=&v", "v"); else MF2_ASM(T_WB_N, "+v", "v"); }
  }
}
#define DLY_AB 0
#define DLY_GD 0
DEVI void delay_ticks(unsigned n) { const u64 t = __builtin_amdgcn_s_memrealtime() + n; while (__builtin_amdgcn_s_memrealtime() < t) __builtin_amdgcn_s_sleep(4); }
#define NOPS2(n, c0, c1) asm volatile("s_nop " #n : "+v"(c0), "+v"(c1))
#define NOPS4(n, c0, c1, c2, c3) asm volatile("s_nop " #n : "+v"(c0), "+v"(c1), "+v"(c2), "+v"(c3))
constexpr int NVS = 5;

template <bool SP> DEVI int swz(int row) { return SP ? (((row & 1) << 1) | (row & 12)) : (((row & 3) << 2) | ((row >> 2) & 3)); }
template <int NSL, bool SP, class SRC, class PUT>
DEVI void load_slabs(unsigned l0, int w, int lane, int rbase, int rstep, int col0, int colt, SRC src, PUT put) {
  constexpr int NB = (NSL + 3) / 4;
  auto stage = [&](int bt) {
#pragma unroll
    for (int j = 0; j < 16; ++j) {
      const int sl = bt * 4 + (j >> 2), blk = w * 4 + (j & 3), pos = blk * 64 + lane;
      if (sl < NSL) glds16(src(sl, pos >> 4, (pos & 15) ^ swz<SP>(pos >> 4)), l0 + (bt & 1) * CH_STG + (j >> 2) * 16384 + blk * 1024);
    }
  };
  stage(0);
#pragma unroll
  for (int bt = 0; bt < NB; ++bt) {
    if (bt + 1 < NB) {
      stage(bt + 1);
      if ((bt + 2) * 4 <= NSL) asm volatile("s_waitcnt vmcnt(16)\n\ts_barrier" ::: "memory");
      else asm volatile("s_waitcnt vmcnt(8)\n\ts_barrier" ::: "memory");
    } else asm volatile("s_waitcnt vmcnt(0)\n\ts_barrier" ::: "memory");
#pragma unroll
    for (int j = 0; j < 4; ++j)
#pragma unroll
      for (int tt = 0; tt < 2; ++tt) {
        if (bt * 4 + j < NSL) {
          s16x4 r[4];
#pragma unroll
          for (int i = 0; i < 4; ++i) {
            const int row = 32 * (i >> 1) + rbase + rstep * (i & 1), cb = (col0 + colt * tt) * 2;
            r[i] = trread(l0 + (bt & 1) * CH_STG + j * 16384 + row * 256 + ((((cb >> 4) ^ swz<SP>(row)) << 4) | (cb & 15)));
          }
          put(bt * 4 + j, tt, cat8(r[0], r[1]), cat8(r[2], r[3]));
        }
      }
    BAR_LDS();
  }
}
DEVI const u16* head_src(const u16* p0, const u16* p1, bool qk, int h, int sl, int row, int ch) {
  if (sl < 10) return p0 + (size_t)(sl * 64 + row) * HE + h * 128 + ch * 8;
  const int o = qk ? (sl - 10) * 64 * HE + h * 128 : (h * 128 + ((sl - 10) & 1) * 64) * D_ + ((sl - 10) >> 1) * 128;
  return p1 + o + row * (qk ? HE : D_) + ch * 8;
}
DEVI void fill_share(rsrc_t rs, int s, int idx, int t, char* ws) {
  if ((s & 3) != 0 || (s / 4 + 1) * NPRE >= NSEG) return;
  const int c = s / 4;
  const u32x4 sv = {~0u, ~0u, ~0u, ~0u};
  constexpr unsigned n = NPRE * REC / 4096;
  static_assert(NPRE * REC % 4096 == 0, "fill");
  for (unsigned i = idx; i < n; i += NFILL) st16(rs, t * 16, OFF_R + (c + 1) * NPRE * REC + i * 4096, sv);
  BAR_ALL();
  if (t == 0) __hip_atomic_fetch_add((unsigned*)(ws + OFF_FLG) + (64 + c) * 64, 1u, __ATOMIC_RELAXED, __HIP_MEMORY_SCOPE_AGENT);
}

__global__ __launch_bounds__(256, 1) void mega_kernel(const u16* __restrict__ x, const u16* __restrict__ Wq, const u16* __restrict__ Wk,
                                                       const u16* __restrict__ Wv, const u16* __restrict__ Wo, const u16* __restrict__ Wg,
                                                       const u16* __restrict__ Wu, const u16* __restrict__ Wd, u16* __restrict__ out, char* ws) {
  __shared__ __attribute__((aligned(16))) char smem[CH_LDS];
  const unsigned l0 = (unsigned)(uintptr_t)(lds_ptr)smem;
  const int t = threadIdx.x, lane = t & 63, w = __builtin_amdgcn_readfirstlane(t >> 6);
  const int b = blockIdx.x;
  unsigned budget = SPIN_BUDGET;
  const rsrc_t rs = __builtin_amdgcn_make_buffer_rsrc((void*)ws, 0, 0x10000000, 0x00020000);
  char* const rec0 = ws + OFF_R;
  const int g = lane >> 4, c16 = lane & 15, q4 = c16 >> 2, p4 = lane & 3;
  const int gp = 2 * (g & 1) + (g >> 1);
  if (b < 64) {
    const bool roleA = b < 32;
    const int h = b & 31;
    bf16x16 W[20][2];
    if (DLY_AB) delay_ticks(DLY_AB);
    const u16* const p0 = roleA ? Wq : Wv; const u16* const p1 = roleA ? Wk : Wo;
    load_slabs<20, true>(l0, w, lane, 16 * (g >> 1) + 4 * (g & 1) + (q4 & 1) + 8 * (q4 >> 1), 2, 32 * w + 4 * p4, 16,
                   [=](int sl, int row, int ch) { return head_src(p0, p1, roleA, h, sl, row, ch); },
                   [&](int sl, int tt, bf16x8 lo, bf16x8 hi) { W[sl][tt] = __builtin_shufflevector(lo, hi, 0, 1, 2, 3, 4, 5, 6, 7, 8, 9, 10, 11, 12, 13, 14, 15); });
    const int sidx = (lane & 8) ? 0xEEEE : 0x4444;
    const unsigned lmem = l0 + CH_MEM, lx = l0 + CH_X;

    if (roleA) {
      u32x4 kf[2][4], qf[4];
      unsigned seen = 0;
      const unsigned qoff = w == 2 ? 4096 : 0;
      auto fetch_qk = [&](int s) {
        wait_prog(rs, h * 256, s + 1, seen, budget);
        const unsigned so = OFF_QK + (s * 32 + h) * 16384;
#pragma unroll
        for (int i = 0; i < 8; ++i) kf[i >> 2][i & 3] = ld16(rs, lane * 16, so + 8192 + i * 1024);
#pragma unroll
        for (int i = 0; i < 4; ++i) qf[i] = ld16(rs, lane * 16, so + qoff + i * 1024);
      };
      const int lim = w == 0 ? 64 : 16 * (w - 1) + c16;
      for (int s = 0; s < NSEG; ++s) {
        TSDECL;
        char* const rec = rec0 + (size_t)s * REC;
        f32x4 aq0 = {0.f, 0.f, 0.f, 0.f}, aq1 = aq0, ak0 = aq0, ak1 = aq0;
        NOPS2(0, aq0, aq1); NOPS2(0, ak0, ak1);
        poll_mem(rec + R_MEM, lmem, w, lane, budget);
        TS(0);
        BAR_LDS();
        TS(1);
        bf16x8 af[10];
#pragma unroll
        for (int ks = 0; ks < 10; ++ks) af[ks] = asb8(ldsr128(lmem + ks * 1024 + lane * 16));
        __builtin_amdgcn_sched_barrier(0);
#pragma unroll
        for (int ks = 0; ks < 10; ++ks) {
          if (ks < NVS) smf2<false>(aq0, aq1, af[ks], W[ks][0], W[ks][1], sidx); else smf2<true>(aq0, aq1, af[ks], W[ks][0], W[ks][1], sidx);
          smf2<true>(ak0, ak1, af[ks], W[10 + ks][0], W[10 + ks][1], sidx);
        }
        NOPS2(3, aq0, aq1); NOPS2(3, ak0, ak1);
        {
          u32x4 o;
          o[0] = cvtpk(pairsum32(ak0[0], ak1[0]), pairsum32(ak0[1], ak1[1]));
          o[1] = cvtpk(pairsum32(ak0[2], ak1[2]), pairsum32(ak0[3], ak1[3]));
          o[2] = cvtpk(pairsum32(aq0[0], aq1[0]), pairsum32(aq0[1], aq1[1]));
          o[3] = cvtpk(pairsum32(aq0[2], aq1[2]), pairsum32(aq0[3], aq1[3]));
          ldsw128(lx + (32 * w + 16 * (lane >> 5) + c16) * 32 + (g & 1) * 16, o);
        }
        TS(2);
        BAR_LDS();
        TS(3);
        if (w < 3) {
          if (s == 0) fetch_qk(0);
          bf16x8 xf[4];
          const int pp = ((p4 & 1) << 1) | (p4 >> 1);
#pragma unroll
          for (int ks = 0; ks < 4; ++ks) {
            const s16x4 x0 = trread(lx + (32 * ks + 8 * g + q4) * 32 + pp * 8);
            const s16x4 x1 = trread(lx + (32 * ks + 8 * g + 4 + q4) * 32 + pp * 8);
            xf[ks] = cat8(x0, x1);
          }
          f32x4 s0 = {0.f, 0.f, 0.f, 0.f}, s1 = s0, s2 = s0;
#pragma unroll
          for (int ks = 0; ks < 4; ++ks) {
            const bf16x8 bq = w == 0 ? xf[ks] : asb8(qf[ks]);
            s0 = MFMA16(xf[ks], bq, s0);
            s1 = MFMA16(asb8(kf[0][ks]), bq, s1);
            s2 = MFMA16(asb8(kf[1][ks]), bq, s2);
          }
          float v[12];
#pragma unroll
          for (int i = 0; i < 4; ++i) {
            v[i] = g < 2 ? rbf(rbf(s0[i]) * SCALE) : -INFINITY;
            v[4 + i] = (4 * g + i <= lim) ? rbf(rbf(s1[i]) * SCALE) : -INFINITY;
            v[8 + i] = (16 + 4 * g + i <= lim) ? rbf(rbf(s2[i]) * SCALE) : -INFINITY;
          }
          float mx = v[0];
#pragma unroll
          for (int i = 1; i < 12; ++i) mx = fmaxf(mx, v[i]);
          mx = xmax32(xmax16(mx));
          float sum = 0.f;
#pragma unroll
          for (int i = 0; i < 12; ++i) { v[i] = __builtin_amdgcn_exp2f((v[i] - mx) * LOG2E); sum += v[i]; }
          sum = xsum32(xsum16(sum));
          const float inv = __builtin_amdgcn_rcpf(sum);
          u32x4 pt; u32x2 pm;
          pm[0] = nosent(cvtpk(v[0] * inv, v[1] * inv)); pm[1] = nosent(cvtpk(v[2] * inv, v[3] * inv));
          pt[0] = nosent(cvtpk(v[4] * inv, v[5] * inv)); pt[1] = nosent(cvtpk(v[6] * inv, v[7] * inv));
          pt[2] = nosent(cvtpk(v[8] * inv, v[9] * inv)); pt[3] = nosent(cvtpk(v[10] * inv, v[11] * inv));
          if (w == 0) {
            char* pd = rec + R_P + h * 640;
            if (lane & 8) {
              st_sc1_x4(pd + (lane & 7) * 64 + g * 16, pt);
              if (g < 2) st_sc1_x2(pd + 512 + (lane & 7) * 16 + g * 8, pm);
            }
          } else {
            char* pd = rec + R_PT + h * 2560;
            st_sc1_x4(pd + (w - 1) * 1024 + lane * 16, pt);
            if (g < 2) st_sc1_x2(pd + 2048 + (16 * (w - 1) + c16) * 16 + g * 8, pm);
          }
          TS(4); TSW(5);
          if (s + 1 < NSEG) fetch_qk(s + 1);
        }
        wait_fill(rs, s, budget);
        TSFLUSH(s);
      }
    } else {
      for (int s = 0; s < NSEG; ++s) {
        TSDECL;
        char* const rec = rec0 + (size_t)s * REC;
        f32x4 a0 = {0.f, 0.f, 0.f, 0.f}, a1 = a0;
        f32x4 c[5][2];
#pragma unroll
        for (int cb = 0; cb < 5; ++cb) { c[cb][0] = a0; c[cb][1] = a0; NOPS2(0, c[cb][0], c[cb][1]); }
        NOPS2(0, a0, a1);
        poll_mem(rec + R_MEM, lmem, w, lane, budget);
        TS(0);
        BAR_LDS();
        TS(1);
        bf16x8 af[10];
#pragma unroll
        for (int ks = 0; ks < 10; ++ks) af[ks] = asb8(ldsr128(lmem + ks * 1024 + lane * 16));
        __builtin_amdgcn_sched_barrier(0);
#pragma unroll
        for (int ks = 0; ks < 10; ++ks) {
          if (ks < NVS) smf2<false>(a0, a1, af[ks], W[ks][0], W[ks][1], sidx); else smf2<true>(a0, a1, af[ks], W[ks][0], W[ks][1], sidx);
        }
        NOPS2(7, a0, a1);
        {
          u32x2 o;
          o[0] = cvtpk(pairsum32(a0[0], a1[0]), pairsum32(a0[1], a1[1]));
          o[1] = cvtpk(pairsum32(a0[2], a1[2]), pairsum32(a0[3], a1[3]));
          ldsw64(lx + (32 * w + 16 * (lane >> 5) + c16) * 16 + (g & 1) * 8, o);
        }
        TS(2);
        BAR_LDS();
        TS(3);
        bf16x8 va[2];
#pragma unroll
        for (int ks = 0; ks < 2; ++ks) {
          const s16x4 x0 = trread(lx + (64 * ks + 16 * g + 8 * (p4 >> 1) + q4) * 16 + (p4 & 1) * 8);
          const s16x4 x1 = trread(lx + (64 * ks + 16 * g + 8 * (p4 >> 1) + 4 + q4) * 16 + (p4 & 1) * 8);
          va[ks] = cat8(x0, x1);
        }
        char* vd = rec + R_VW + (size_t)(2 * w + (lane >> 5)) * 8192 + h * 256 + c16 * 16 + (g & 1) * 8;
#pragma unroll
        for (int cb = 0; cb < 6; ++cb) {
          if (cb < 5) {
            smf2<true>(c[cb][0], c[cb][1], va[0], W[10 + 2 * cb][0], W[10 + 2 * cb][1], sidx);
            smf2<true>(c[cb][0], c[cb][1], va[1], W[11 + 2 * cb][0], W[11 + 2 * cb][1], sidx);
          }
          if (cb > 0) {
            f32x4 c0 = c[cb - 1][0], c1 = c[cb - 1][1];
            if (cb < 5) NOPS2(3, c0, c1); else NOPS2(7, c0, c1);
            u32x2 o;
            o[0] = nosent(cvtpk(pairsum32(c0[0], c1[0]), pairsum32(c0[1], c1[1])));
            o[1] = nosent(cvtpk(pairsum32(c0[2], c1[2]), pairsum32(c0[3], c1[3])));
            st_sc1_x2(vd + (cb - 1) * 65536, o);
          }
        }
        TS(4); TSW(5);
        wait_fill(rs, s, budget);
        TSFLUSH(s);
      }
    }
  } else if (b < 104) {
    const int ct = b - 64;
    u32x4 uf[8];
    unsigned seen = 0;
    auto fetch_u = [&](int s) {
      wait_prog(rs, (32 + 8 * w + (lane & 7)) * 256, s + 1, seen, budget);
#pragma unroll
      for (int i = 0; i < 8; ++i) uf[i] = ld16(rs, lane * 16, OFF_U + ((s * 40 + ct) * 32 + 8 * w + i) * 1024);
    };
    fetch_u(0);
    const int r = lane & 7, hb = (lane >> 3) & 1;
    const unsigned lred = l0;
    for (int s = 0; s < NSEG - 1; ++s) {
      char* const rec = rec0 + (size_t)s * REC;
      const char* pt = rec + R_P + (8 * w + hb) * 640 + r * 64 + g * 16;
      const char* pm = rec + R_P + (8 * w + 4 * hb + g) * 640 + 512 + r * 16;
      const char* vp = rec + R_VW + (size_t)ct * 8192 + 8 * w * 256 + lane * 16;
      u32x4 t0 = {~0u, ~0u, ~0u, ~0u}, t1 = t0, t2 = t0, t3 = t0, m0 = t0, v0 = t0, v1 = t0;
      u64 k0 = ~0ull, k1 = ~0ull, k2 = ~0ull, k3 = ~0ull, k4 = ~0ull, k5 = ~0ull, k6 = ~0ull, sv;
      TSDECL;
      for (;;) {
        asm volatile("s_mov_b64 %[sv], exec\n\t"
                     "s_mov_b64 exec, %[k5]\n\tglobal_load_dwordx4 %[v0], %[vp], off sc1\n\t"
                     "s_mov_b64 exec, %[k6]\n\tglobal_load_dwordx4 %[v1], %[vp], off offset:1024 sc1\n\t"
                     "s_mov_b64 exec, %[k0]\n\tglobal_load_dwordx4 %[t0], %[pt], off sc1\n\t"
                     "s_mov_b64 exec, %[k1]\n\tglobal_load_dwordx4 %[t1], %[pt], off offset:1280 sc1\n\t"
                     "s_mov_b64 exec, %[k2]\n\tglobal_load_dwordx4 %[t2], %[pt], off offset:2560 sc1\n\t"
                     "s_mov_b64 exec, %[k3]\n\tglobal_load_dwordx4 %[t3], %[pt], off offset:3840 sc1\n\t"
                     "s_mov_b64 exec, %[k4]\n\tglobal_load_dwordx4 %[m0], %[pm], off sc1\n\t"
                     "s_mov_b64 exec, %[sv]\n\ts_waitcnt vmcnt(0)"
                     : [t0] "+v"(t0), [t1] "+v"(t1), [t2] "+v"(t2), [t3] "+v"(t3), [m0] "+v"(m0), [v0] "+v"(v0), [v1] "+v"(v1), [sv] "=&s"(sv)
                     : [k0] "s"(k0), [k1] "s"(k1), [k2] "s"(k2), [k3] "s"(k3), [k4] "s"(k4), [k5] "s"(k5), [k6] "s"(k6),
                       [pt] "v"(pt), [pm] "v"(pm), [vp] "v"(vp) : "memory");
        k0 = PENDING(t0); k1 = PENDING(t1); k2 = PENDING(t2); k3 = PENDING(t3); k4 = PENDING(m0); k5 = PENDING(v0); k6 = PENDING(v1);
        if ((k0 | k1 | k2 | k3 | k4 | k5 | k6) == 0 || budget == 0) break;
        --budget;
      }
      TS(0);
      f32x4 ae = {0.f, 0.f, 0.f, 0.f}, ao = ae;
      ae = MFMA16(asb8(uf[0]), asb8(t0), ae); ao = MFMA16(asb8(uf[1]), asb8(t0), ao);
      ae = MFMA16(asb8(uf[2]), asb8(t1), ae); ao = MFMA16(asb8(uf[3]), asb8(t1), ao);
      ae = MFMA16(asb8(uf[4]), asb8(t2), ae); ao = MFMA16(asb8(uf[5]), asb8(t2), ao);
      ae = MFMA16(asb8(uf[6]), asb8(t3), ae); ao = MFMA16(asb8(uf[7]), asb8(t3), ao);
      ae = MFMA16(asb8(v0), asb8(m0), ae);    ao = MFMA16(asb8(v1), asb8(m0), ao);
      const f32x4 sel = hb ? ao : ae;
      ldsw128(lred + ((w * 2 + hb) * 8 + r) * 64 + g * 16, __builtin_bit_cast(u32x4, sel));
      TS(1);
      BAR_LDS();
      TS(2);
      if (w == 0) {
        const int cq = lane >> 3;
        float x0 = 0.f, x1 = 0.f;
#pragma unroll
        for (int i = 0; i < 8; ++i) { const u32x2 z = ldsr64(lred + (i * 8 + r) * 64 + cq * 8); x0 += __uint_as_float(z[0]); x1 += __uint_as_float(z[1]); }
        st_sc1_x1(rec + REC + R_MEM + ct * 256 + (cq >> 2) * 128 + r * 16 + (cq & 3) * 4, nosent(cvtpk(x0, x1)));
      }
      TS(3); TSW(4);
      TSFLUSH(s);
      if (s + 2 < NSEG) fetch_u(s + 1);
      wait_fill(rs, s, budget);
    }
  } else if (b < 168) {
    const bool qk = b < 136;
    const int h = (b - 104) & 31;
    bf16x8 W[20][2][2];
    const u16* const p0 = qk ? Wq : Wv; const u16* const p1 = qk ? Wk : Wo;
    load_slabs<20, false>(l0, w, lane, 8 * g + q4, 4, 32 * w + 4 * p4, 16,
                   [=](int sl, int row, int ch) { return head_src(p0, p1, qk, h, sl, row, ch); },
                   [&](int sl, int tt, bf16x8 lo, bf16x8 hi) { W[sl][tt][0] = lo; W[sl][tt][1] = hi; });
    auto stageX = [&](int s) {
#pragma unroll
      for (int j = 0; j < 10; ++j) glds16(ws + OFF_XN + (size_t)s * XN_SEG + (w * 10 + j) * 1024 + lane * 16, l0 + (s & 1) * XN_SEG + (w * 10 + j) * 1024);
    };
    const unsigned lv = l0 + 2 * XN_SEG;
    const unsigned pword = OFF_FLG + (b - 104) * 256;
    stageX(0);
    asm volatile("s_waitcnt vmcnt(0)" ::: "memory");
    for (int s = 0; s < NSEG; ++s) {
      TSDECL;
      if (s <= 4) asm volatile("s_waitcnt vmcnt(0)" ::: "memory");
      BAR_LDS();
      if (t == 0 && s >= 1) st4(rs, 0, pword, s <= 4 ? s : s - 1);
      if (s + 1 < NSEG) stageX(s + 1);
      TS(0);
      const unsigned lxb = l0 + (s & 1) * XN_SEG + lane * 16;
      bf16x8 xa = asb8(ldsr128(lxb)), xb = asb8(ldsr128(lxb + 1024));
      if (qk) {
        f32x4 acc[2][4];
#pragma unroll
        for (int ks = 0; ks < 20; ++ks) {
          bf16x8 na = xa, nb = xb;
          if (ks + 1 < 20) { na = asb8(ldsr128(lxb + (ks + 1) * 2048)); nb = asb8(ldsr128(lxb + (ks + 1) * 2048 + 1024)); }
          __builtin_amdgcn_sched_barrier(0);
          const int sl = ks >> 1, kk = ks & 1;
#define QKSTEP(F) \
          if (sl < NVS) { mf2<false, true, F>(acc[0][0], acc[0][1], W[sl][0][kk], W[sl][1][kk], xa); mf2<false, true, F>(acc[1][0], acc[1][1], W[sl][0][kk], W[sl][1][kk], xb); } \
          else { mf2<true, true, F>(acc[0][0], acc[0][1], W[sl][0][kk], W[sl][1][kk], xa); mf2<true, true, F>(acc[1][0], acc[1][1], W[sl][0][kk], W[sl][1][kk], xb); } \
          mf2<true, true, F>(acc[0][2], acc[0][3], W[10 + sl][0][kk], W[10 + sl][1][kk], xa); mf2<true, true, F>(acc[1][2], acc[1][3], W[10 + sl][0][kk], W[10 + sl][1][kk], xb);
          if (ks == 0) { QKSTEP(true) } else { QKSTEP(false) }
          __builtin_amdgcn_sched_barrier(0);
          xa = na; xb = nb;
        }
        NOPS4(3, acc[0][0], acc[0][1], acc[0][2], acc[0][3]); NOPS4(3, acc[1][0], acc[1][1], acc[1][2], acc[1][3]);
        TS(1);
        asm volatile("s_waitcnt vmcnt(0)" ::: "memory");
        TS(2);
        const unsigned so = OFF_QK + (s * 32 + h) * 16384 + w * 1024, vo = (gp * 16 + c16) * 16;
#pragma unroll
        for (int mt = 0; mt < 2; ++mt) {
          st16(rs, vo, so + mt * 4096, widen4(acc[mt][0], acc[mt][1]));
          st16(rs, vo, so + 8192 + mt * 4096, widen4(acc[mt][2], acc[mt][3]));
        }
      } else {
        f32x4 av[2][2];
#pragma unroll
        for (int ks = 0; ks < 20; ++ks) {
          bf16x8 na = xa, nb = xb;
          if (ks + 1 < 20) { na = asb8(ldsr128(lxb + (ks + 1) * 2048)); nb = asb8(ldsr128(lxb + (ks + 1) * 2048 + 1024)); }
          __builtin_amdgcn_sched_barrier(0);
          const int sl = ks >> 1, kk = ks & 1;
#define VSTEP(F) \
          if (sl < NVS) { mf2<false, true, F>(av[0][0], av[0][1], W[sl][0][kk], W[sl][1][kk], xa); mf2<false, true, F>(av[1][0], av[1][1], W[sl][0][kk], W[sl][1][kk], xb); } \
          else { mf2<true, true, F>(av[0][0], av[0][1], W[sl][0][kk], W[sl][1][kk], xa); mf2<true, true, F>(av[1][0], av[1][1], W[sl][0][kk], W[sl][1][kk], xb); }
          if (ks == 0) { VSTEP(true) } else { VSTEP(false) }
          __builtin_amdgcn_sched_barrier(0);
          xa = na; xb = nb;
        }
        NOPS4(7, av[0][0], av[0][1], av[1][0], av[1][1]);
        ldsw128(lv + (w * 2) * 1024 + (gp * 16 + c16) * 16, widen4(av[0][0], av[0][1]));
        ldsw128(lv + (w * 2 + 1) * 1024 + (gp * 16 + c16) * 16, widen4(av[1][0], av[1][1]));
        BAR_LDS();
        bf16x8 vf[4][2];
#pragma unroll
        for (int i = 0; i < 8; ++i) vf[i >> 1][i & 1] = asb8(ldsr128(lv + i * 1024 + lane * 16));
        TS(1);
        asm volatile("s_waitcnt vmcnt(0)" ::: "memory");
        TS(2);
        __builtin_amdgcn_sched_barrier(0);
        f32x4 cu[5][2][2];
#pragma unroll
        for (int cb = 0; cb < 6; ++cb) {
          if (cb < 5) {
#pragma unroll
            for (int ks = 0; ks < 4; ++ks) {
              const int sl = 10 + 2 * cb + (ks >> 1), kk = ks & 1;
              if (ks == 0) { mf2<true, false, true>(cu[cb][0][0], cu[cb][0][1], W[sl][0][kk], W[sl][1][kk], vf[ks][0]); mf2<true, false, true>(cu[cb][1][0], cu[cb][1][1], W[sl][0][kk], W[sl][1][kk], vf[ks][1]); }
              else { mf2<true, false, false>(cu[cb][0][0], cu[cb][0][1], W[sl][0][kk], W[sl][1][kk], vf[ks][0]); mf2<true, false, false>(cu[cb][1][0], cu[cb][1][1], W[sl][0][kk], W[sl][1][kk], vf[ks][1]); }
            }
          }
          if (cb > 0) {
            f32x4 x0 = cu[cb - 1][0][0], x1 = cu[cb - 1][0][1], y0 = cu[cb - 1][1][0], y1 = cu[cb - 1][1][1];
            if (cb < 5) NOPS4(0, x0, x1, y0, y1); else NOPS4(7, x0, x1, y0, y1);
            const unsigned so = OFF_U + ((s * 40 + (cb - 1) * 8 + 2 * w) * 32 + h) * 1024;
            u32x4 o;
            o[0] = cvtpk(x0[0], x0[1]); o[1] = cvtpk(x0[2], x0[3]); o[2] = cvtpk(y0[0], y0[1]); o[3] = cvtpk(y0[2], y0[3]);
            st16(rs, lane * 16, so, o);
            o[0] = cvtpk(x1[0], x1[1]); o[1] = cvtpk(x1[2], x1[3]); o[2] = cvtpk(y1[0], y1[1]); o[3] = cvtpk(y1[2], y1[3]);
            st16(rs, lane * 16, so + 32768, o);
          }
        }
      }
      TS(3);
      TSFLUSH(s);
    }
    BAR_ALL();
    if (t == 0) st4(rs, 0, pword, NSEG);
  } else if (b < 208) {
    const int ct = b - 168;
    u32x4 uf[8];
    unsigned seen = 0;
    auto fetch_u = [&](int s) {
      wait_prog(rs, (32 + 8 * w + (lane & 7)) * 256, s + 1, seen, budget);
#pragma unroll
      for (int i = 0; i < 8; ++i) uf[i] = ld16(rs, lane * 16, OFF_U + ((s * 40 + ct) * 32 + 8 * w + i) * 1024);
    };
    for (int s = 0; s < NSEG; ++s) {
      TSDECL;
      const unsigned ro = OFF_R + s * REC;
      u32x2 xr = {0u, 0u};
      if (w < 2) xr = *(const u32x2*)(x + (size_t)(s * 32 + 16 * w + c16) * D_ + ct * 16 + 4 * g);
      if (s + 1 < NSEG) wait_word(rs, ro + R_PT + (8 * w + 7) * 2560 + 1024, budget);
      if (s == 0) fetch_u(0);
      TS(0);
      u32x4 v[22];
      poll_n<22>(rs, v,
                 [=](int i) { return i < 16 ? lane * 16 : (i < 20 ? g * 2560 + c16 * 16 : lane * 16); },
                 [=](int i) { return i < 16 ? ro + R_PT + (8 * w + (i >> 1)) * 2560 + (i & 1) * 1024
                                            : (i < 20 ? ro + R_PT + (8 * w + 4 * ((i - 16) >> 1)) * 2560 + 2048 + (i & 1) * 256
                                                      : ro + R_VW + ct * 8192 + (8 * w + 4 * (i - 20)) * 256); }, budget);
      TS(1);
      f32x4 a0 = {0.f, 0.f, 0.f, 0.f}, a1 = a0;
#pragma unroll
      for (int i = 0; i < 8; ++i) { a0 = MFMA16(asb8(uf[i]), asb8(v[2 * i]), a0); a1 = MFMA16(asb8(uf[i]), asb8(v[2 * i + 1]), a1); }
#pragma unroll
      for (int i = 0; i < 2; ++i) { a0 = MFMA16(asb8(v[20 + i]), asb8(v[16 + 2 * i]), a0); a1 = MFMA16(asb8(v[20 + i]), asb8(v[17 + 2 * i]), a1); }
      const unsigned lred = l0 + (s & 1) * 8192;
      ldsw128(lred + (w * 2) * 1024 + lane * 16, __builtin_bit_cast(u32x4, a0));
      ldsw128(lred + (w * 2 + 1) * 1024 + lane * 16, __builtin_bit_cast(u32x4, a1));
      BAR_LDS();
      if (w < 2) {
        f32x4 z = __builtin_bit_cast(f32x4, ldsr128(lred + w * 1024 + lane * 16));
#pragma unroll
        for (int i = 1; i < 4; ++i) z += __builtin_bit_cast(f32x4, ldsr128(lred + (i * 2 + w) * 1024 + lane * 16));
        u32x2 o;
        o[0] = nosent(cvtpk(bflo(xr[0]) + rbf(z[0]), bfhi(xr[0]) + rbf(z[1])));
        o[1] = nosent(cvtpk(bflo(xr[1]) + rbf(z[2]), bfhi(xr[1]) + rbf(z[3])));
        st8(rs, c16 * 32 + g * 8, ro + R_H + ct * 1024 + w * 512, o);
      }
      TS(2);
      TSFLUSH(s);
      if (s + 1 < NSEG) fetch_u(s + 1);
      fill_share(rs, s, ct, t, ws);
      wait_fill(rs, s, budget);
    }
  } else if (b < 235) {
    const int j = b - 208;
    if (DLY_GD) delay_ticks(DLY_GD);
    bf16x8 W[20][2];
    load_slabs<10, false>(l0, w, lane, 8 * g + q4, 4, 16 * w + 4 * p4, 64,
                   [=](int sl, int row, int ch) { return Wg + (ch < 8 ? (ptrdiff_t)0 : Wu - Wg) + (size_t)(sl * 64 + row) * F_ + 64 * j + (ch & 7) * 8; },
                   [&](int sl, int tt, bf16x8 lo, bf16x8 hi) { W[2 * sl][tt] = lo; W[2 * sl + 1][tt] = hi; });
    const int row = 8 * w + (c16 >> 1), half = lane & 1;
    const unsigned hvo = g * 1024 + row * 32 + half * 16;
    u32x4 v[10];
#pragma unroll
    for (int i = 0; i < 10; ++i) v[i] = u32x4{~0u, ~0u, ~0u, ~0u};
    for (int s = 0; s < NSEG; ++s) {
      TSDECL;
      const unsigned ro = OFF_R + s * REC;
      unsigned pend = 0;
#pragma unroll
      for (int i = 0; i < 10; ++i) pend |= (PENDING(v[i]) != 0 ? 1u : 0u) << i;
      if (pend == 0x3FFu && s + 1 < NSEG) wait_word(rs, ro + R_H + w * 256, budget);
      TS(0);
      if (pend) poll_n<10>(rs, v, [=](int) { return hvo; }, [=](int i) { return ro + R_H + i * 4096; }, budget, pend);
      TS(1);
      ln_fast<10>(v, [](float z) { return xsum32(xsum16(xsum1(z))); });
      const unsigned lh = l0 + (s & 1) * XN_SEG;
#pragma unroll
      for (int i = 0; i < 10; ++i)
        ldsw128(lh + ((2 * i + (g >> 1)) * 2 + (row >> 4)) * 1024 + ((2 * (g & 1) + half) * 16 + (row & 15)) * 16, v[i]);
      wait_fill(rs, s, budget);
      if (s + 1 < NSEG) {
#pragma unroll
        for (int i = 0; i < 10; ++i) v[i] = ld16(rs, hvo, ro + REC + R_H + i * 4096);
      }
      BAR_LDS();
      TS(2);
      f32x4 acc[2][2];
      bf16x8 xf[20][2];
#pragma unroll
      for (int ks = 0; ks < 4; ++ks) { xf[ks][0] = asb8(ldsr128(lh + ks * 2048 + lane * 16)); xf[ks][1] = asb8(ldsr128(lh + ks * 2048 + 1024 + lane * 16)); }
#pragma unroll
      for (int ks = 0; ks < 20; ++ks) {
        if (ks + 4 < 20) { xf[ks + 4][0] = asb8(ldsr128(lh + (ks + 4) * 2048 + lane * 16)); xf[ks + 4][1] = asb8(ldsr128(lh + (ks + 4) * 2048 + 1024 + lane * 16)); }
        __builtin_amdgcn_sched_barrier(0);
        if (ks == 0) { mf2<true, true, true>(acc[0][0], acc[0][1], W[ks][0], W[ks][1], xf[ks][0]); mf2<true, true, true>(acc[1][0], acc[1][1], W[ks][0], W[ks][1], xf[ks][1]); }
        else { mf2<true, true, false>(acc[0][0], acc[0][1], W[ks][0], W[ks][1], xf[ks][0]); mf2<true, true, false>(acc[1][0], acc[1][1], W[ks][0], W[ks][1], xf[ks][1]); }
        __builtin_amdgcn_sched_barrier(0);
      }
      NOPS4(7, acc[0][0], acc[0][1], acc[1][0], acc[1][1]);
      f32x4 a[2];
#pragma unroll
      for (int mt = 0; mt < 2; ++mt)
#pragma unroll
        for (int i = 0; i < 4; ++i) { const float gt = rbf(acc[mt][0][i]), up = rbf(acc[mt][1][i]); a[mt][i] = (gt > 0.f ? gt : gt * 0.f) * up; }
      st16(rs, (g & 1) * 1024 + ((2 * (w & 1) + (g >> 1)) * 16 + c16) * 16, ro + R_ACT + (2 * j + (w >> 1)) * 2048, nosent4(widen4(a[0], a[1])));
      TS(3);
      TSFLUSH(s);
    }
  } else if (b < 255) {
    const int d = b - 235;
    if (DLY_GD) delay_ticks(DLY_GD);
#pragma unroll
    for (int i = 0; i < 27; ++i) glds16(Wd + (size_t)((w * 27 + i) * 16 + (lane >> 2)) * D_ + 32 * d + (lane & 3) * 8, l0 + (w * 27 + i) * 1024);
    BAR_ALL();
    const int k0 = w < 2 ? 14 * w : 13 * w + 2;
    bf16x8 W[14][2];
#pragma unroll
    for (int kk = 0; kk < 14; ++kk)
#pragma unroll
      for (int nt = 0; nt < 2; ++nt) {
        const int ks = (kk == 13 && w >= 2) ? k0 : k0 + kk;
        const unsigned a = l0 + (32 * ks + 8 * g + q4) * 64 + (16 * nt + 4 * p4) * 2;
        W[kk][nt] = cat8(trread(a), trread(a + 256));
      }
    BAR_LDS();
    for (int s = 0; s < NSEG; ++s) {
      TSDECL;
      const unsigned ro = OFF_R + s * REC;
      if (s + 1 < NSEG) wait_word(rs, ro + R_ACT + k0 * 2048, budget);
      TS(0);
      u32x4 v[28];
      poll_n<28>(rs, v, [=](int) { return lane * 16; }, [=](int i) { return ro + R_ACT + ((i >= 26 && w >= 2) ? k0 * 2 + (i - 26) : k0 * 2 + i) * 1024; }, budget);
      u32x4 hr = {0u, 0u, 0u, 0u};
      if (w < 2) hr = ld16(rs, (g & 1) * 1024 + c16 * 32 + (g >> 1) * 16, ro + R_H + 2 * d * 1024 + w * 512);
      TS(1);
      f32x4 acc[2][2];
#pragma unroll
      for (int kk = 0; kk < 13; ++kk) {
        if (kk == 0) { mf2<true, true, true>(acc[0][0], acc[0][1], W[kk][0], W[kk][1], asb8(v[2 * kk])); mf2<true, true, true>(acc[1][0], acc[1][1], W[kk][0], W[kk][1], asb8(v[2 * kk + 1])); }
        else { mf2<true, true, false>(acc[0][0], acc[0][1], W[kk][0], W[kk][1], asb8(v[2 * kk])); mf2<true, true, false>(acc[1][0], acc[1][1], W[kk][0], W[kk][1], asb8(v[2 * kk + 1])); }
      }
      if (w < 2) { mf2<true, true, false>(acc[0][0], acc[0][1], W[13][0], W[13][1], asb8(v[26])); mf2<true, true, false>(acc[1][0], acc[1][1], W[13][0], W[13][1], asb8(v[27])); }
      NOPS4(7, acc[0][0], acc[0][1], acc[1][0], acc[1][1]);
      const unsigned lred = l0 + (s & 1) * 16384;
#pragma unroll
      for (int i = 0; i < 4; ++i) ldsw128(lred + (w * 4 + i) * 1024 + lane * 16, __builtin_bit_cast(u32x4, acc[i >> 1][i & 1]));
      BAR_LDS();
      if (w < 2) {
        f32x4 z0 = __builtin_bit_cast(f32x4, ldsr128(lred + (2 * w) * 1024 + lane * 16)), z1 = __builtin_bit_cast(f32x4, ldsr128(lred + (2 * w + 1) * 1024 + lane * 16));
#pragma unroll
        for (int i = 1; i < 4; ++i) {
          z0 += __builtin_bit_cast(f32x4, ldsr128(lred + (i * 4 + 2 * w) * 1024 + lane * 16));
          z1 += __builtin_bit_cast(f32x4, ldsr128(lred + (i * 4 + 2 * w + 1) * 1024 + lane * 16));
        }
        const u32x4 o = widen4(z0, z1);
        u32x4 z;
#pragma unroll
        for (int i = 0; i < 4; ++i) z[i] = cvtpk(bflo(hr[i]) + bflo(o[i]), bfhi(hr[i]) + bfhi(o[i]));
        *(u32x4*)(out + (size_t)(s * 32 + 16 * w + c16) * D_ + 32 * d + 16 * (g & 1) + 8 * (g >> 1)) = z;
      }
      TS(2);
      TSFLUSH(s);
      fill_share(rs, s, 40 + d, t, ws);
      wait_fill(rs, s, budget);
    }
  }
}

extern "C" void kernel_launch(void* const* d_in, const int* in_sizes, int n_in, void* d_out, int out_size, void* d_ws, size_t ws_size,
                              hipStream_t stream) {
  const u16* x = (const u16*)d_in[0];
  char* ws = (char*)d_ws;
  prep_kernel<<<128 + FILL_BLOCKS + 1, 256, 0, stream>>>(x, (const u16*)d_in[1], ws);
  mega_kernel<<<256, 256, 0, stream>>>(x, (const u16*)d_in[2], (const u16*)d_in[3], (const u16*)d_in[4], (const u16*)d_in[5],
                                       (const u16*)d_in[8], (const u16*)d_in[9], (const u16*)d_in[10], (u16*)d_out, ws);
}
```

```cpp
#include <hip/hip_runtime.h>
#include <hip/hip_bf16.h>
#include <stdint.h>

typedef __bf16 bf16x8 __attribute__((ext_vector_type(8)));
typedef __bf16 bf16x16 __attribute__((ext_vector_type(16)));
typedef __bf16 bf16x4 __attribute__((ext_vector_type(4)));
typedef float f32x4 __attribute__((ext_vector_type(4)));
typedef unsigned u32x4 __attribute__((ext_vector_type(4)));
typedef unsigned u32x2 __attribute__((ext_vector_type(2)));
typedef short s16x4 __attribute__((ext_vector_type(4)));
typedef __attribute__((address_space(3))) char* lds_ptr;
typedef unsigned short u16;

#define DEVI __device__ __forceinline__
#define LDBITS "sc1"
#define STBITS "sc1"

constexpr int S_ = 2048, D_ = 640, H_ = 32, E_ = 128, HE = 4096, F_ = 1728, NSEG = 64;
constexpr size_t OFF_XN = 0;
constexpr size_t OFF_Q = OFF_XN + (size_t)S_ * D_ * 2;
constexpr size_t OFF_K = OFF_Q + (size_t)S_ * HE * 2;
constexpr size_t OFF_U = OFF_K + (size_t)S_ * HE * 2;
constexpr size_t OFF_KM = OFF_U + (size_t)NSEG * 40 * 32 * 1024;
constexpr size_t OFF_PT = OFF_KM + (size_t)NSEG * 32 * 4096;
constexpr size_t OFF_H = OFF_PT + (size_t)NSEG * 32 * 3072;
constexpr size_t OFF_HN = OFF_H + (size_t)S_ * D_ * 2;
constexpr size_t OFF_ACT = OFF_HN + (size_t)S_ * D_ * 2;
constexpr size_t OFF_X = OFF_ACT + (size_t)S_ * F_ * 2;
constexpr size_t MEM_STEP = 10240, P_STEP = 20480, VW_STEP = 327680;
constexpr size_t OFF_MEM = OFF_X;
constexpr size_t OFF_P = OFF_MEM + NSEG * MEM_STEP;
constexpr size_t OFF_VW = OFF_P + NSEG * P_STEP;
constexpr size_t OFF_END = OFF_VW + NSEG * VW_STEP;
static_assert(OFF_X % 256 == 0 && OFF_U % 256 == 0 && OFF_KM % 256 == 0 && OFF_PT % 256 == 0, "align");
static_assert(OFF_END <= (size_t)256 * 1024 * 1024, "ws");

typedef __bf16 bf16x2 __attribute__((ext_vector_type(2)));
typedef float f32x2 __attribute__((ext_vector_type(2)));
DEVI unsigned cvtpk(float lo, float hi) { const f32x2 v = {lo, hi}; return __builtin_bit_cast(unsigned, __builtin_convertvector(v, bf16x2)); }
DEVI float bflo(unsigned u) { return __uint_as_float(u << 16); }
DEVI float bfhi(unsigned u) { return __uint_as_float(u & 0xffff0000u); }
DEVI float rbf(float x) { return bflo(cvtpk(x, x)); }
DEVI unsigned nosent(unsigned u) { return u < 0xFFFFFFFEu ? u : 0xFFFFFFFEu; }
DEVI void glds16(const void* g, unsigned lds_base) {
  unsigned sv;
  asm volatile("s_mov_b32 %0, m0\n\ts_mov_b32 m0, %2\n\ts_nop 0\n\tglobal_load_lds_dwordx4 %1, off\n\ts_mov_b32 m0, %0" : "=&s"(sv) : "v"(g), "s"(lds_base) : "memory");
}
DEVI s16x4 trread(unsigned a) { return __builtin_amdgcn_ds_read_tr16_b64_v4i16((__attribute__((address_space(3))) s16x4*)(uintptr_t)a); }
DEVI bf16x8 cat8(s16x4 a, s16x4 b) { return __builtin_bit_cast(bf16x8, __builtin_shufflevector(a, b, 0, 1, 2, 3, 4, 5, 6, 7)); }
DEVI u32x4 ldsr128(unsigned a) { return *(__attribute__((address_space(3))) u32x4*)(uintptr_t)a; }
DEVI u32x2 ldsr64(unsigned a) { return *(__attribute__((address_space(3))) u32x2*)(uintptr_t)a; }
DEVI void ldsw128(unsigned a, u32x4 v) { *(__attribute__((address_space(3))) u32x4*)(uintptr_t)a = v; }
DEVI void ldsw64(unsigned a, u32x2 v) { *(__attribute__((address_space(3))) u32x2*)(uintptr_t)a = v; }
DEVI bf16x8 asb8(u32x4 v) { return __builtin_bit_cast(bf16x8, v); }
#define MFMA16(a, b, c) __builtin_amdgcn_mfma_f32_16x16x32_bf16(a, b, c, 0, 0, 0)
#define SMF(a, b, c, idx) __builtin_amdgcn_smfmac_f32_16x16x64_bf16(a, b, c, idx, 0, 0)
#define BAR_ALL() asm volatile("s_waitcnt vmcnt(0) lgkmcnt(0)\n\ts_barrier" ::: "memory")
#define BAR_LDS() asm volatile("s_waitcnt lgkmcnt(0)\n\ts_barrier" ::: "memory")
DEVI float xmax16(float x) { auto r = __builtin_amdgcn_permlane16_swap(__float_as_uint(x), __float_as_uint(x), false, false); return fmaxf(__uint_as_float(r[0]), __uint_as_float(r[1])); }
DEVI float xmax32(float x) { auto r = __builtin_amdgcn_permlane32_swap(__float_as_uint(x), __float_as_uint(x), false, false); return fmaxf(__uint_as_float(r[0]), __uint_as_float(r[1])); }
DEVI float xsum16(float x) { auto r = __builtin_amdgcn_permlane16_swap(__float_as_uint(x), __float_as_uint(x), false, false); return __uint_as_float(r[0]) + __uint_as_float(r[1]); }
DEVI float xsum32(float x) { auto r = __builtin_amdgcn_permlane32_swap(__float_as_uint(x), __float_as_uint(x), false, false); return __uint_as_float(r[0]) + __uint_as_float(r[1]); }
DEVI float pairsum32(float a, float b) { auto r = __builtin_amdgcn_permlane32_swap(__float_as_uint(a), __float_as_uint(b), false, false); return __uint_as_float(r[0]) + __uint_as_float(r[1]); }
DEVI void st_sc1_x4(void* p, u32x4 v) { asm volatile("global_store_dwordx4 %0, %1, off " STBITS "\n\ts_nop 1" ::"v"(p), "v"(v) : "memory"); }
DEVI void st_sc1_x2(void* p, u32x2 v) { asm volatile("global_store_dwordx2 %0, %1, off " STBITS "\n\ts_nop 1" ::"v"(p), "v"(v) : "memory"); }
DEVI void st_sc1_x1(void* p, unsigned v) { asm volatile("global_store_dword %0, %1, off " STBITS "\n\ts_nop 1" ::"v"(p), "v"(v) : "memory"); }
DEVI unsigned umax4(u32x4 v) { return max(max(v[0], v[1]), max(v[2], v[3])); }

DEVI void ln_row(const unsigned* __restrict__ src, unsigned* __restrict__ dst, int lane) {
  unsigned v[5]; float f[10]; float s = 0.f;
#pragma unroll
  for (int i = 0; i < 5; ++i) { v[i] = src[lane + 64 * i]; f[2 * i] = bflo(v[i]); f[2 * i + 1] = bfhi(v[i]); s += f[2 * i] + f[2 * i + 1]; }
#pragma unroll
  for (int o = 1; o < 64; o <<= 1) s += __shfl_xor(s, o);
  const float mean = s * (1.0f / 640.0f);
  float q = 0.f;
#pragma unroll
  for (int i = 0; i < 10; ++i) { float d = f[i] - mean; q += d * d; }
#pragma unroll
  for (int o = 1; o < 64; o <<= 1) q += __shfl_xor(q, o);
  const float mb = rbf(mean), vb = rbf(q * (1.0f / 640.0f));
  const float sd = rbf(sqrtf(rbf(vb + 1e-05f)));
#pragma unroll
  for (int i = 0; i < 5; ++i) { float a = rbf(f[2 * i] - mb), b = rbf(f[2 * i + 1] - mb); dst[lane + 64 * i] = cvtpk(a / sd, b / sd); }
}

constexpr int FILL_BLOCKS = 1024;
__global__ __launch_bounds__(256) void prep_kernel(const unsigned* __restrict__ x, const u16* __restrict__ mem0, char* __restrict__ ws) {
  const int b = blockIdx.x, t = threadIdx.x;
  if (b < 512) {
    const int row = b * 4 + (t >> 6);
    ln_row(x + (size_t)row * 320, (unsigned*)(ws + OFF_XN) + (size_t)row * 320, t & 63);
  } else if (b < 512 + FILL_BLOCKS) {
    u32x4* p = (u32x4*)(ws + OFF_MEM + MEM_STEP);
    const size_t n = (OFF_END - OFF_MEM - MEM_STEP) / 16;
    const u32x4 s = {~0u, ~0u, ~0u, ~0u};
    for (size_t i = (size_t)(b - 512) * 256 + t; i < n; i += (size_t)FILL_BLOCKS * 256) p[i] = s;
  } else {
    for (int i = t; i < 640; i += 256) {
      const int ct = i >> 4, bb = (i >> 3) & 1, r = i & 7;
      u32x4 v = *(const u32x4*)(mem0 + r * 640 + ct * 16 + bb * 8);
      v[0] = nosent(v[0]); v[1] = nosent(v[1]); v[2] = nosent(v[2]); v[3] = nosent(v[3]);
      *(u32x4*)(ws + OFF_MEM + (size_t)i * 16) = v;
    }
  }
}
__global__ __launch_bounds__(256) void ln2_kernel(char* __restrict__ ws) {
  const int row = blockIdx.x * 4 + (threadIdx.x >> 6);
  ln_row((const unsigned*)(ws + OFF_H) + (size_t)row * 320, (unsigned*)(ws + OFF_HN) + (size_t)row * 320, threadIdx.x & 63);
}

template <int BM>
DEVI void stageA(const u16* __restrict__ A, int lda, int row0, int k0, unsigned lds, int w, int lane) {
#pragma unroll
  for (int j = 0; j < BM / 32; ++j) {
    const int blk = w * (BM / 32) + j, pos = blk * 64 + lane, row = pos >> 3, ch = (pos & 7) ^ (row & 7);
    glds16(A + (size_t)(row0 + row) * lda + k0 + ch * 8, lds + blk * 1024);
  }
}
DEVI int swzB(int row) { return ((row & 3) << 2) | ((row >> 2) & 3); }
DEVI void stageB(const u16* __restrict__ B0, const u16* __restrict__ B1, int ldb, int k0, unsigned lds, int w, int lane) {
#pragma unroll
  for (int j = 0; j < 4; ++j) {
    const int blk = w * 4 + j, pos = blk * 64 + lane, row = pos >> 4, ch = (pos & 15) ^ swzB(row);
    const u16* src = (ch < 8 ? B0 + ch * 8 : B1 + (ch - 8) * 8) + (size_t)(k0 + row) * ldb;
    glds16(src, lds + blk * 1024);
  }
}
DEVI bf16x8 fragA(unsigned lds, int mt, int ks, int lane) {
  const int row = mt * 16 + (lane & 15), ch = 4 * ks + (lane >> 4);
  return asb8(ldsr128(lds + row * 128 + ((ch ^ (row & 7)) << 4)));
}
DEVI bf16x8 fragB(unsigned lds, int nt, int ks, int lane) {
  const int g = lane >> 4, q = (lane & 15) >> 2, p = lane & 3;
  const int r0 = 32 * ks + 8 * g + q, r1 = r0 + 4, ch = 2 * nt + (p >> 1);
  const s16x4 a = trread(lds + r0 * 256 + ((ch ^ swzB(r0)) << 4) + 8 * (p & 1));
  const s16x4 b = trread(lds + r1 * 256 + ((ch ^ swzB(r1)) << 4) + 8 * (p & 1));
  return cat8(a, b);
}
template <int MT, bool TR>
DEVI void mma_slab(f32x4 (&acc)[MT][4], unsigned la, unsigned lb, int mt0, int nta, int ntb, int lane) {
#pragma unroll
  for (int ks = 0; ks < 2; ++ks) {
    bf16x8 a[MT], b[4];
#pragma unroll
    for (int m = 0; m < MT; ++m) a[m] = fragA(la, mt0 + m, ks, lane);
    b[0] = fragB(lb, nta, ks, lane); b[1] = fragB(lb, nta + 1, ks, lane);
    b[2] = fragB(lb, ntb, ks, lane); b[3] = fragB(lb, ntb + 1, ks, lane);
#pragma unroll
    for (int m = 0; m < MT; ++m)
#pragma unroll
      for (int n = 0; n < 4; ++n) acc[m][n] = TR ? MFMA16(b[n], a[m], acc[m][n]) : MFMA16(a[m], b[n], acc[m][n]);
  }
}
DEVI u32x4 widen(unsigned a0, unsigned a1, unsigned b0, unsigned b1) {
  auto r0 = __builtin_amdgcn_permlane16_swap(a0, b0, false, false);
  auto r1 = __builtin_amdgcn_permlane16_swap(a1, b1, false, false);
  u32x4 o; o[0] = r0[0]; o[1] = r1[0]; o[2] = r0[1]; o[3] = r1[1]; return o;
}

template <int MODE, int BM>
__global__ __launch_bounds__(256, 2) void gemm_kernel(const u16* __restrict__ A, const u16* __restrict__ B0, const u16* __restrict__ B1,
                                                       u16* __restrict__ C0, u16* __restrict__ C1, const u16* __restrict__ R) {
  constexpr int MT = BM / 32;
  constexpr int ASZ = BM * 128, STG = ASZ + 16384;
  constexpr int LDA = MODE == 2 ? F_ : D_, NK = (MODE == 2 ? F_ : D_) / 64;
  constexpr int LDB_ = MODE == 0 ? HE : (MODE == 1 ? F_ : D_);
  constexpr int NMT = S_ / BM;
  __shared__ __attribute__((aligned(16))) char smem[2 * STG];
  const unsigned l0 = (unsigned)(uintptr_t)(lds_ptr)smem;
  const int t = threadIdx.x, lane = t & 63, w = __builtin_amdgcn_readfirstlane(t >> 6), wm = w >> 1, wn = w & 1;
  const int mi = blockIdx.x % NMT, ni = blockIdx.x / NMT;
  const int row0 = mi * BM;
  const u16 *b0, *b1; u16* C; int ccol;
  if (MODE == 0) { const bool isk = ni >= 32; const u16* Bm = isk ? B1 : B0; ccol = (ni & 31) * 128; b0 = Bm + ccol; b1 = b0 + 64; C = isk ? C1 : C0; }
  else if (MODE == 1) { ccol = ni * 64; b0 = B0 + ccol; b1 = B1 + ccol; C = C0; }
  else { ccol = ni * 128; b0 = B0 + ccol; b1 = b0 + 64; C = C0; }
  const int nta = MODE == 1 ? 2 * wn : 4 * wn, ntb = MODE == 1 ? 4 + 2 * wn : 4 * wn + 2;
  f32x4 acc[MT][4];
#pragma unroll
  for (int m = 0; m < MT; ++m)
#pragma unroll
    for (int n = 0; n < 4; ++n) acc[m][n] = f32x4{0.f, 0.f, 0.f, 0.f};
  stageA<BM>(A, LDA, row0, 0, l0, w, lane);
  stageB(b0, b1, LDB_, 0, l0 + ASZ, w, lane);
  for (int kt = 0; kt < NK; ++kt) {
    BAR_ALL();
    const unsigned cur = l0 + (kt & 1) * STG, nxt = l0 + ((kt + 1) & 1) * STG;
    if (kt + 1 < NK) { stageA<BM>(A, LDA, row0, (kt + 1) * 64, nxt, w, lane); stageB(b0, b1, LDB_, (kt + 1) * 64, nxt + ASZ, w, lane); }
    mma_slab<MT, true>(acc, cur, cur + ASZ, wm * MT, nta, ntb, lane);
  }
  const int g = lane >> 4, coff = 16 * (g & 1) + 8 * (g >> 1);
#pragma unroll
  for (int m = 0; m < MT; ++m) {
    const int row = row0 + (wm * MT + m) * 16 + (lane & 15);
    if (MODE == 0) {
#pragma unroll
      for (int pr = 0; pr < 2; ++pr) {
        const f32x4 x = acc[m][2 * pr], y = acc[m][2 * pr + 1];
        const u32x4 o = widen(cvtpk(x[0], x[1]), cvtpk(x[2], x[3]), cvtpk(y[0], y[1]), cvtpk(y[2], y[3]));
        *(u32x4*)(C + (size_t)row * HE + ccol + wn * 64 + pr * 32 + coff) = o;
      }
    } else if (MODE == 1) {
      float a[8];
#pragma unroll
      for (int i = 0; i < 8; ++i) {
        const float gt = rbf(acc[m][i >> 2][i & 3]), up = rbf(acc[m][2 + (i >> 2)][i & 3]);
        a[i] = (gt > 0.f ? gt : gt * 0.f) * up;
      }
      const u32x4 o = widen(cvtpk(a[0], a[1]), cvtpk(a[2], a[3]), cvtpk(a[4], a[5]), cvtpk(a[6], a[7]));
      *(u32x4*)(C + (size_t)row * F_ + ccol + wn * 32 + coff) = o;
    } else {
#pragma unroll
      for (int pr = 0; pr < 2; ++pr) {
        const f32x4 x = acc[m][2 * pr], y = acc[m][2 * pr + 1];
        const u32x4 o = widen(cvtpk(x[0], x[1]), cvtpk(x[2], x[3]), cvtpk(y[0], y[1]), cvtpk(y[2], y[3]));
        const size_t off = (size_t)row * D_ + ccol + wn * 64 + pr * 32 + coff;
        const u32x4 r = *(const u32x4*)(R + off);
        u32x4 z;
#pragma unroll
        for (int i = 0; i < 4; ++i) z[i] = cvtpk(bflo(r[i]) + bflo(o[i]), bfhi(r[i]) + bfhi(o[i]));
        *(u32x4*)(C + off) = z;
      }
    }
  }
}

__global__ __launch_bounds__(256, 2) void gemmVU_kernel(const u16* __restrict__ XN, const u16* __restrict__ Wv, const u16* __restrict__ Wo, char* __restrict__ U) {
  constexpr int STG = 32768;
  __shared__ __attribute__((aligned(16))) char smem[2 * STG];
  const unsigned l0 = (unsigned)(uintptr_t)(lds_ptr)smem;
  const int t = threadIdx.x, lane = t & 63, w = __builtin_amdgcn_readfirstlane(t >> 6), wm = w >> 1, wn = w & 1;
  const int mi = blockIdx.x & 15, h = blockIdx.x >> 4, row0 = mi * 128;
  const u16* b0 = Wv + h * 128;
  f32x4 acc[4][4];
#pragma unroll
  for (int m = 0; m < 4; ++m)
#pragma unroll
    for (int n = 0; n < 4; ++n) acc[m][n] = f32x4{0.f, 0.f, 0.f, 0.f};
  stageA<128>(XN, D_, row0, 0, l0, w, lane);
  stageB(b0, b0 + 64, HE, 0, l0 + 16384, w, lane);
  for (int kt = 0; kt < 10; ++kt) {
    BAR_ALL();
    const unsigned cur = l0 + (kt & 1) * STG, nxt = l0 + ((kt + 1) & 1) * STG;
    if (kt + 1 < 10) { stageA<128>(XN, D_, row0, (kt + 1) * 64, nxt, w, lane); stageB(b0, b0 + 64, HE, (kt + 1) * 64, nxt + 16384, w, lane); }
    mma_slab<4, true>(acc, cur, cur + 16384, wm * 4, 4 * wn, 4 * wn + 2, lane);
  }
  BAR_ALL();
  const u16* wo = Wo + (size_t)h * 128 * D_;
  stageB(wo, wo + 64, D_, 0, l0 + 16384, w, lane);
  stageB(wo, wo + 64, D_, 64, l0 + STG + 16384, w, lane);
  {
    const int g = lane >> 4;
#pragma unroll
    for (int m = 0; m < 4; ++m)
#pragma unroll
      for (int n = 0; n < 4; ++n) {
        const int row = (wm * 4 + m) * 16 + (lane & 15), ch = 2 * n + (g >> 1);
        u32x2 v; v[0] = cvtpk(acc[m][n][0], acc[m][n][1]); v[1] = cvtpk(acc[m][n][2], acc[m][n][3]);
        ldsw64(l0 + wn * STG + row * 128 + ((ch ^ (row & 7)) << 4) + 8 * (g & 1), v);
      }
  }
  for (int nb = 0; nb < 5; ++nb) {
    BAR_ALL();
#pragma unroll
    for (int m = 0; m < 4; ++m)
#pragma unroll
      for (int n = 0; n < 4; ++n) acc[m][n] = f32x4{0.f, 0.f, 0.f, 0.f};
    mma_slab<4, false>(acc, l0, l0 + 16384, wm * 4, 4 * wn, 4 * wn + 2, lane);
    mma_slab<4, false>(acc, l0 + STG, l0 + STG + 16384, wm * 4, 4 * wn, 4 * wn + 2, lane);
    BAR_LDS();
    if (nb + 1 < 5) {
      const u16* wn_ = wo + (nb + 1) * 128;
      stageB(wn_, wn_ + 64, D_, 0, l0 + 16384, w, lane);
      stageB(wn_, wn_ + 64, D_, 64, l0 + STG + 16384, w, lane);
    }
#pragma unroll
    for (int sl = 0; sl < 2; ++sl)
#pragma unroll
      for (int n = 0; n < 4; ++n) {
        const int seg = mi * 4 + wm * 2 + sl, ct = nb * 8 + wn * 4 + n;
        const f32x4 x = acc[2 * sl][n], y = acc[2 * sl + 1][n];
        u32x4 o; o[0] = cvtpk(x[0], x[1]); o[1] = cvtpk(x[2], x[3]); o[2] = cvtpk(y[0], y[1]); o[3] = cvtpk(y[2], y[3]);
        *(u32x4*)(U + ((size_t)(seg * 40 + ct) * 32 + h) * 1024 + lane * 16) = o;
      }
  }
}

constexpr float SCALE = 0.08838834764831845f, LOG2E = 1.4426950408889634f;
constexpr unsigned SPIN_BUDGET = 1u << 22;
constexpr int CH_STG = 65536;
constexpr int CH_MEM = 2 * CH_STG, CH_X = CH_MEM + 10240, CH_LDS = CH_X + 4096;

#define TS(i)
#define TSW(i)
#define TSDECL
#define TSFLUSH(s)
typedef unsigned long long u64;
#define PENDING(v) __builtin_amdgcn_ballot_w64(umax4(v) == ~0u)
DEVI void poll_mem(const char* memp, unsigned lmem, int w, int lane, unsigned& budget) {
  const char* p0 = memp + w * 2560 + lane * 16;
  u32x4 a0 = {~0u, ~0u, ~0u, ~0u}, a1 = a0, a2 = {0u, 0u, 0u, 0u};
  u64 m0 = ~0ull, m1 = ~0ull, m2 = 0xFFFFFFFFull, sv;
  for (;;) {
    asm volatile("s_mov_b64 %[sv], exec\n\t"
                 "s_mov_b64 exec, %[m0]\n\tglobal_load_dwordx4 %[a0], %[p0], off " LDBITS "\n\t"
                 "s_mov_b64 exec, %[m1]\n\tglobal_load_dwordx4 %[a1], %[p0], off offset:1024 " LDBITS "\n\t"
                 "s_mov_b64 exec, %[m2]\n\tglobal_load_dwordx4 %[a2], %[p0], off offset:2048 " LDBITS "\n\t"
                 "s_mov_b64 exec, %[sv]\n\ts_waitcnt vmcnt(0)"
                 : [a0] "+v"(a0), [a1] "+v"(a1), [a2] "+v"(a2), [sv] "=&s"(sv)
                 : [m0] "s"(m0), [m1] "s"(m1), [m2] "s"(m2), [p0] "v"(p0) : "memory");
    m0 = PENDING(a0); m1 = PENDING(a1); m2 = PENDING(a2);
    if ((m0 | m1 | m2) == 0 || budget == 0) break;
    --budget;
  }
  ldsw128(lmem + w * 2560 + lane * 16, a0);
  ldsw128(lmem + w * 2560 + 1024 + lane * 16, a1);
  if (lane < 32) ldsw128(lmem + w * 2560 + 2048 + lane * 16, a2);
}
template <bool AG>
DEVI void smf2(f32x4& c0, f32x4& c1, bf16x8 a, const bf16x16& w0, const bf16x16& w1, int idx) {
  if (AG) asm volatile("v_smfmac_f32_16x16x64_bf16 %0, %2, %3, %5\n\tv_smfmac_f32_16x16x64_bf16 %1, %2, %4, %5" : "+v"(c0), "+v"(c1) : "v"(a), "a"(w0), "a"(w1), "v"(idx));
  else asm volatile("v_smfmac_f32_16x16x64_bf16 %0, %2, %3, %5\n\tv_smfmac_f32_16x16x64_bf16 %1, %2, %4, %5" : "+v"(c0), "+v"(c1) : "v"(a), "v"(w0), "v"(w1), "v"(idx));
}
#define DLY_AB 0
#define DLY_C 0
DEVI void delay_until(u64 t) { while (__builtin_amdgcn_s_memrealtime() < t) __builtin_amdgcn_s_sleep(1); }
#define NOPS2(n, c0, c1) asm volatile("s_nop " #n : "+v"(c0), "+v"(c1))
constexpr int NVS = 5;

__global__ __launch_bounds__(256, 1) void chain_kernel(const u16* __restrict__ Wq, const u16* __restrict__ Wk, const u16* __restrict__ Wv,
                                                        const u16* __restrict__ Wo, char* ws) {
  __shared__ __attribute__((aligned(16))) char smem[CH_LDS];
  const unsigned l0 = (unsigned)(uintptr_t)(lds_ptr)smem;
  const int t = threadIdx.x, lane = t & 63, w = __builtin_amdgcn_readfirstlane(t >> 6);
  const int b = blockIdx.x;
  if (b >= 104) return;
  unsigned budget = SPIN_BUDGET;
  u64 tlast = 0;
  char* const memx = ws + OFF_MEM; char* const px = ws + OFF_P; char* const vwx = ws + OFF_VW;
  const int g = lane >> 4, q4 = (lane & 15) >> 2, p4 = lane & 3;

  if (b < 64) {
    const bool roleA = b < 32;
    const int h = b & 31;
    bf16x16 W[20][2];
    auto slab_src = [&](int sl, int row) -> const u16* {
      if (roleA) return (sl < 10 ? Wq : Wk) + (size_t)((sl % 10) * 64 + row) * HE + h * 128;
      if (sl < 10) return Wv + (size_t)(sl * 64 + row) * HE + h * 128;
      return Wo + (size_t)(h * 128 + ((sl - 10) & 1) * 64 + row) * D_ + ((sl - 10) >> 1) * 128;
    };
    auto stage_batch = [&](int bt) {
#pragma unroll
      for (int j = 0; j < 16; ++j) {
        const int sl = bt * 4 + (j >> 2), blk = w * 4 + (j & 3), pos = blk * 64 + lane;
        glds16(slab_src(sl, pos >> 4) + (pos & 15) * 8, l0 + (bt & 1) * CH_STG + (j >> 2) * 16384 + blk * 1024);
      }
    };
    stage_batch(0);
#pragma unroll
    for (int bt = 0; bt < 5; ++bt) {
      if (bt + 1 < 5) { stage_batch(bt + 1); asm volatile("s_waitcnt vmcnt(16)\n\ts_barrier" ::: "memory"); }
      else asm volatile("s_waitcnt vmcnt(0)\n\ts_barrier" ::: "memory");
#pragma unroll
      for (int j = 0; j < 4; ++j)
#pragma unroll
        for (int tt = 0; tt < 2; ++tt) {
          s16x4 r[4];
#pragma unroll
          for (int i = 0; i < 4; ++i) {
            const int row = 32 * (i >> 1) + 16 * (g >> 1) + 4 * (g & 1) + 2 * (i & 1) + (q4 & 1) + 8 * (q4 >> 1);
            r[i] = trread(l0 + (bt & 1) * CH_STG + j * 16384 + row * 256 + (32 * w + 16 * tt + 4 * p4) * 2);
          }
          const bf16x8 lo = cat8(r[0], r[1]), hi = cat8(r[2], r[3]);
          W[bt * 4 + j][tt] = __builtin_shufflevector(lo, hi, 0, 1, 2, 3, 4, 5, 6, 7, 8, 9, 10, 11, 12, 13, 14, 15);
        }
      BAR_LDS();
    }
    const int sidx = (lane & 8) ? 0xEEEE : 0x4444;
    const unsigned lmem = l0 + CH_MEM, lx = l0 + CH_X;

    if (roleA) {
      const u16* Kt = (const u16*)(ws + OFF_K) + h * 128 + (size_t)(lane & 15) * HE + 8 * g;
      u32x4 kf[2][4];
      if (w == 0) {
#pragma unroll
        for (int i = 0; i < 8; ++i) kf[i >> 2][i & 3] = *(const u32x4*)(Kt + (size_t)(16 * (i >> 2)) * HE + 32 * (i & 3));
      }
      for (int s = 0; s < NSEG; ++s) {
        TSDECL;
        f32x4 aq0 = {0.f, 0.f, 0.f, 0.f}, aq1 = aq0, ak0 = aq0, ak1 = aq0;
        NOPS2(0, aq0, aq1); NOPS2(0, ak0, ak1);
        if (DLY_AB && s > 0) delay_until(tlast + DLY_AB);
        poll_mem(memx + (size_t)s * MEM_STEP, lmem, w, lane, budget);
        TS(0);
        BAR_LDS();
        TS(1);
        bf16x8 af[10];
#pragma unroll
        for (int ks = 0; ks < 10; ++ks) af[ks] = asb8(ldsr128(lmem + ks * 1024 + lane * 16));
        __builtin_amdgcn_sched_barrier(0);
#pragma unroll
        for (int ks = 0; ks < 10; ++ks) {
          if (ks < NVS) smf2<false>(aq0, aq1, af[ks], W[ks][0], W[ks][1], sidx); else smf2<true>(aq0, aq1, af[ks], W[ks][0], W[ks][1], sidx);
          smf2<true>(ak0, ak1, af[ks], W[10 + ks][0], W[10 + ks][1], sidx);
        }
        NOPS2(3, aq0, aq1); NOPS2(3, ak0, ak1);
        {
          u32x4 o;
          o[0] = cvtpk(pairsum32(ak0[0], ak1[0]), pairsum32(ak0[1], ak1[1]));
          o[1] = cvtpk(pairsum32(ak0[2], ak1[2]), pairsum32(ak0[3], ak1[3]));
          o[2] = cvtpk(pairsum32(aq0[0], aq1[0]), pairsum32(aq0[1], aq1[1]));
          o[3] = cvtpk(pairsum32(aq0[2], aq1[2]), pairsum32(aq0[3], aq1[3]));
          ldsw128(lx + (32 * w + 16 * (lane >> 5) + (lane & 15)) * 32 + (g & 1) * 16, o);
        }
        TS(2);
        BAR_LDS();
        TS(3);
        if (DLY_AB) tlast = __builtin_amdgcn_s_memrealtime() + 40;
        if (w == 0) {
          bf16x8 xf[4];
          const int pp = ((p4 & 1) << 1) | (p4 >> 1);
#pragma unroll
          for (int ks = 0; ks < 4; ++ks) {
            const s16x4 x0 = trread(lx + (32 * ks + 8 * g + q4) * 32 + pp * 8);
            const s16x4 x1 = trread(lx + (32 * ks + 8 * g + 4 + q4) * 32 + pp * 8);
            xf[ks] = cat8(x0, x1);
          }
          f32x4 s0 = {0.f, 0.f, 0.f, 0.f}, s1 = s0, s2 = s0;
#pragma unroll
          for (int ks = 0; ks < 4; ++ks) {
            s0 = MFMA16(xf[ks], xf[ks], s0);
            s1 = MFMA16(asb8(kf[0][ks]), xf[ks], s1);
            s2 = MFMA16(asb8(kf[1][ks]), xf[ks], s2);
          }
          float v[12];
#pragma unroll
          for (int i = 0; i < 4; ++i) {
            v[i] = g < 2 ? rbf(rbf(s0[i]) * SCALE) : -INFINITY;
            v[4 + i] = rbf(rbf(s1[i]) * SCALE);
            v[8 + i] = rbf(rbf(s2[i]) * SCALE);
          }
          float mx = v[0];
#pragma unroll
          for (int i = 1; i < 12; ++i) mx = fmaxf(mx, v[i]);
          mx = xmax32(xmax16(mx));
          float sum = 0.f;
#pragma unroll
          for (int i = 0; i < 12; ++i) { v[i] = __builtin_amdgcn_exp2f((v[i] - mx) * LOG2E); sum += v[i]; }
          sum = xsum32(xsum16(sum));
          const float inv = __builtin_amdgcn_rcpf(sum);
          u32x4 pt; u32x2 pm;
          pm[0] = nosent(cvtpk(v[0] * inv, v[1] * inv)); pm[1] = nosent(cvtpk(v[2] * inv, v[3] * inv));
          pt[0] = nosent(cvtpk(v[4] * inv, v[5] * inv)); pt[1] = nosent(cvtpk(v[6] * inv, v[7] * inv));
          pt[2] = nosent(cvtpk(v[8] * inv, v[9] * inv)); pt[3] = nosent(cvtpk(v[10] * inv, v[11] * inv));
          char* pd = px + (size_t)s * P_STEP + h * 640;
          if (lane & 8) {
            st_sc1_x4(pd + (lane & 7) * 64 + g * 16, pt);
            if (g < 2) st_sc1_x2(pd + 512 + (lane & 7) * 16 + g * 8, pm);
          }
          TS(4); TSW(5);
          char* km = ws + OFF_KM + ((size_t)s * 32 + h) * 4096 + lane * 16;
#pragma unroll
          for (int ks = 0; ks < 4; ++ks) *(u32x4*)(km + ks * 1024) = __builtin_bit_cast(u32x4, xf[ks]);
          if (s + 1 < NSEG) {
#pragma unroll
            for (int i = 0; i < 8; ++i) kf[i >> 2][i & 3] = *(const u32x4*)(Kt + (size_t)((s + 1) * 32 + 16 * (i >> 2)) * HE + 32 * (i & 3));
          }
        }
        TSFLUSH(s);
      }
    } else {
      for (int s = 0; s < NSEG; ++s) {
        TSDECL;
        f32x4 a0 = {0.f, 0.f, 0.f, 0.f}, a1 = a0;
        f32x4 c[5][2];
#pragma unroll
        for (int cb = 0; cb < 5; ++cb) { c[cb][0] = a0; c[cb][1] = a0; NOPS2(0, c[cb][0], c[cb][1]); }
        NOPS2(0, a0, a1);
        if (DLY_AB && s > 0) delay_until(tlast + DLY_AB);
        poll_mem(memx + (size_t)s * MEM_STEP, lmem, w, lane, budget);
        TS(0);
        BAR_LDS();
        TS(1);
        bf16x8 af[10];
#pragma unroll
        for (int ks = 0; ks < 10; ++ks) af[ks] = asb8(ldsr128(lmem + ks * 1024 + lane * 16));
        __builtin_amdgcn_sched_barrier(0);
#pragma unroll
        for (int ks = 0; ks < 10; ++ks) {
          if (ks < NVS) smf2<false>(a0, a1, af[ks], W[ks][0], W[ks][1], sidx); else smf2<true>(a0, a1, af[ks], W[ks][0], W[ks][1], sidx);
        }
        NOPS2(7, a0, a1);
        {
          u32x2 o;
          o[0] = cvtpk(pairsum32(a0[0], a1[0]), pairsum32(a0[1], a1[1]));
          o[1] = cvtpk(pairsum32(a0[2], a1[2]), pairsum32(a0[3], a1[3]));
          ldsw64(lx + (32 * w + 16 * (lane >> 5) + (lane & 15)) * 16 + (g & 1) * 8, o);
        }
        TS(2);
        BAR_LDS();
        TS(3);
        if (DLY_AB) tlast = __builtin_amdgcn_s_memrealtime() + 40;
        bf16x8 va[2];
#pragma unroll
        for (int ks = 0; ks < 2; ++ks) {
          const s16x4 x0 = trread(lx + (64 * ks + 16 * g + 8 * (p4 >> 1) + q4) * 16 + (p4 & 1) * 8);
          const s16x4 x1 = trread(lx + (64 * ks + 16 * g + 8 * (p4 >> 1) + 4 + q4) * 16 + (p4 & 1) * 8);
          va[ks] = cat8(x0, x1);
        }
        char* vd = vwx + (size_t)s * VW_STEP + (size_t)(2 * w + (lane >> 5)) * 8192 + h * 256 + (lane & 15) * 16 + (g & 1) * 8;
#pragma unroll
        for (int cb = 0; cb < 6; ++cb) {
          if (cb < 5) {
            smf2<true>(c[cb][0], c[cb][1], va[0], W[10 + 2 * cb][0], W[10 + 2 * cb][1], sidx);
            smf2<true>(c[cb][0], c[cb][1], va[1], W[11 + 2 * cb][0], W[11 + 2 * cb][1], sidx);
          }
          if (cb > 0) {
            f32x4 c0 = c[cb - 1][0], c1 = c[cb - 1][1];
            if (cb < 5) NOPS2(3, c0, c1); else NOPS2(7, c0, c1);
            u32x2 o;
            o[0] = nosent(cvtpk(pairsum32(c0[0], c1[0]), pairsum32(c0[1], c1[1])));
            o[1] = nosent(cvtpk(pairsum32(c0[2], c1[2]), pairsum32(c0[3], c1[3])));
            st_sc1_x2(vd + (cb - 1) * 65536, o);
          }
        }
        TS(4); TSW(5);
        TSFLUSH(s);
      }
    }
  } else {
    const int ct = b - 64;
    const char* up = ws + OFF_U + ((size_t)ct * 32 + 8 * w) * 1024 + lane * 16;
    u32x4 uf[8];
#pragma unroll
    for (int i = 0; i < 8; ++i) uf[i] = *(const u32x4*)(up + i * 1024);
    const int r = lane & 7, hb = (lane >> 3) & 1;
    const unsigned lred = l0;
    for (int s = 0; s < NSEG - 1; ++s) {
      const char* pt = px + (size_t)s * P_STEP + (8 * w + hb) * 640 + r * 64 + g * 16;
      const char* pm = px + (size_t)s * P_STEP + (8 * w + 4 * hb + g) * 640 + 512 + r * 16;
      const char* vp = vwx + (size_t)s * VW_STEP + (size_t)ct * 8192 + 8 * w * 256 + lane * 16;
      u32x4 t0 = {~0u, ~0u, ~0u, ~0u}, t1 = t0, t2 = t0, t3 = t0, m0 = t0, v0 = t0, v1 = t0;
      u64 k0 = ~0ull, k1 = ~0ull, k2 = ~0ull, k3 = ~0ull, k4 = ~0ull, k5 = ~0ull, k6 = ~0ull, sv;
      TSDECL;
      if (DLY_C && s > 0) delay_until(tlast + DLY_C);
      for (;;) {
        asm volatile("s_mov_b64 %[sv], exec\n\t"
                     "s_mov_b64 exec, %[k5]\n\tglobal_load_dwordx4 %[v0], %[vp], off " LDBITS "\n\t"
                     "s_mov_b64 exec, %[k6]\n\tglobal_load_dwordx4 %[v1], %[vp], off offset:1024 " LDBITS "\n\t"
                     "s_mov_b64 exec, %[k0]\n\tglobal_load_dwordx4 %[t0], %[pt], off " LDBITS "\n\t"
                     "s_mov_b64 exec, %[k1]\n\tglobal_load_dwordx4 %[t1], %[pt], off offset:1280 " LDBITS "\n\t"
                     "s_mov_b64 exec, %[k2]\n\tglobal_load_dwordx4 %[t2], %[pt], off offset:2560 " LDBITS "\n\t"
                     "s_mov_b64 exec, %[k3]\n\tglobal_load_dwordx4 %[t3], %[pt], off offset:3840 " LDBITS "\n\t"
                     "s_mov_b64 exec, %[k4]\n\tglobal_load_dwordx4 %[m0], %[pm], off " LDBITS "\n\t"
                     "s_mov_b64 exec, %[sv]\n\ts_waitcnt vmcnt(0)"
                     : [t0] "+v"(t0), [t1] "+v"(t1), [t2] "+v"(t2), [t3] "+v"(t3), [m0] "+v"(m0), [v0] "+v"(v0), [v1] "+v"(v1), [sv] "=&s"(sv)
                     : [k0] "s"(k0), [k1] "s"(k1), [k2] "s"(k2), [k3] "s"(k3), [k4] "s"(k4), [k5] "s"(k5), [k6] "s"(k6),
                       [pt] "v"(pt), [pm] "v"(pm), [vp] "v"(vp) : "memory");
        k0 = PENDING(t0); k1 = PENDING(t1); k2 = PENDING(t2); k3 = PENDING(t3); k4 = PENDING(m0); k5 = PENDING(v0); k6 = PENDING(v1);
        if ((k0 | k1 | k2 | k3 | k4 | k5 | k6) == 0 || budget == 0) break;
        --budget;
      }
      TS(0);
      f32x4 ae = {0.f, 0.f, 0.f, 0.f}, ao = ae;
      ae = MFMA16(asb8(uf[0]), asb8(t0), ae); ao = MFMA16(asb8(uf[1]), asb8(t0), ao);
      ae = MFMA16(asb8(uf[2]), asb8(t1), ae); ao = MFMA16(asb8(uf[3]), asb8(t1), ao);
      ae = MFMA16(asb8(uf[4]), asb8(t2), ae); ao = MFMA16(asb8(uf[5]), asb8(t2), ao);
      ae = MFMA16(asb8(uf[6]), asb8(t3), ae); ao = MFMA16(asb8(uf[7]), asb8(t3), ao);
      ae = MFMA16(asb8(v0), asb8(m0), ae);    ao = MFMA16(asb8(v1), asb8(m0), ao);
      const f32x4 sel = hb ? ao : ae;
      ldsw128(lred + ((w * 2 + hb) * 8 + r) * 64 + g * 16, __builtin_bit_cast(u32x4, sel));
      TS(1);
      BAR_LDS();
      TS(2);
      if (DLY_C) tlast = __builtin_amdgcn_s_memrealtime() + 10;
      if (w == 0) {
        const int cq = lane >> 3;
        float x0 = 0.f, x1 = 0.f;
#pragma unroll
        for (int i = 0; i < 8; ++i) { const u32x2 z = ldsr64(lred + (i * 8 + r) * 64 + cq * 8); x0 += __uint_as_float(z[0]); x1 += __uint_as_float(z[1]); }
        st_sc1_x1(memx + (size_t)(s + 1) * MEM_STEP + ct * 256 + (cq >> 2) * 128 + r * 16 + (cq & 3) * 4, nosent(cvtpk(x0, x1)));
      }
      TS(3); TSW(4);
      TSFLUSH(s);
      if (s + 2 < NSEG) {
#pragma unroll
        for (int i = 0; i < 8; ++i) uf[i] = *(const u32x4*)(up + (size_t)(s + 1) * (40 * 32 * 1024) + i * 1024);
      }
    }
  }
}

__global__ __launch_bounds__(256) void tokP_kernel(char* __restrict__ ws) {
  const int t = threadIdx.x, lane = t & 63, w = t >> 6;
  const int seg = blockIdx.x >> 3, h = (blockIdx.x & 7) * 4 + w;
  const int g = lane >> 4, c = lane & 15;
  const u16* Q = (const u16*)(ws + OFF_Q) + (size_t)(seg * 32 + c) * HE + h * 128 + 8 * g;
  const u16* K = (const u16*)(ws + OFF_K) + (size_t)(seg * 32 + c) * HE + h * 128 + 8 * g;
  const char* km = ws + OFF_KM + ((size_t)seg * 32 + h) * 4096 + lane * 16;
  f32x4 s[3][2];
#pragma unroll
  for (int i = 0; i < 6; ++i) s[i >> 1][i & 1] = f32x4{0.f, 0.f, 0.f, 0.f};
#pragma unroll
  for (int ks = 0; ks < 4; ++ks) {
    const bf16x8 m = asb8(*(const u32x4*)(km + ks * 1024));
    const bf16x8 k0 = asb8(*(const u32x4*)(K + 32 * ks)), k1 = asb8(*(const u32x4*)(K + (size_t)16 * HE + 32 * ks));
    const bf16x8 q0 = asb8(*(const u32x4*)(Q + 32 * ks)), q1 = asb8(*(const u32x4*)(Q + (size_t)16 * HE + 32 * ks));
    s[0][0] = MFMA16(m, q0, s[0][0]); s[0][1] = MFMA16(m, q1, s[0][1]);
    s[1][0] = MFMA16(k0, q0, s[1][0]); s[1][1] = MFMA16(k0, q1, s[1][1]);
    s[2][1] = MFMA16(k1, q1, s[2][1]);
  }
  char* pd = ws + OFF_PT + ((size_t)seg * 32 + h) * 3072;
#pragma unroll
  for (int qt = 0; qt < 2; ++qt) {
    float v[12];
#pragma unroll
    for (int i = 0; i < 4; ++i) {
      v[i] = g < 2 ? rbf(rbf(s[0][qt][i]) * SCALE) : -INFINITY;
      v[4 + i] = (4 * g + i <= 16 * qt + c) ? rbf(rbf(s[1][qt][i]) * SCALE) : -INFINITY;
      v[8 + i] = (16 + 4 * g + i <= 16 * qt + c) ? rbf(rbf(s[2][qt][i]) * SCALE) : -INFINITY;
    }
    float mx = v[0];
#pragma unroll
    for (int i = 1; i < 12; ++i) mx = fmaxf(mx, v[i]);
    mx = xmax32(xmax16(mx));
    float sum = 0.f;
#pragma unroll
    for (int i = 0; i < 12; ++i) { v[i] = __builtin_amdgcn_exp2f((v[i] - mx) * LOG2E); sum += v[i]; }
    sum = xsum32(xsum16(sum));
    const float inv = 1.0f / sum;
    u32x4 pt; u32x2 pm;
    pm[0] = cvtpk(v[0] * inv, v[1] * inv); pm[1] = cvtpk(v[2] * inv, v[3] * inv);
    pt[0] = cvtpk(v[4] * inv, v[5] * inv); pt[1] = cvtpk(v[6] * inv, v[7] * inv);
    pt[2] = cvtpk(v[8] * inv, v[9] * inv); pt[3] = cvtpk(v[10] * inv, v[11] * inv);
    *(u32x4*)(pd + qt * 1024 + lane * 16) = pt;
    *(u32x2*)(pd + 2048 + qt * 512 + lane * 8) = pm;
  }
}

__global__ __launch_bounds__(256) void tokO_kernel(const u16* __restrict__ x, char* __restrict__ ws) {
  const int t = threadIdx.x, lane = t & 63, w = t >> 6;
  const int seg = blockIdx.x / 5, ct0 = (blockIdx.x % 5) * 8 + 2 * w;
  const int g = lane >> 4, c = lane & 15;
  const char* up = ws + OFF_U + (size_t)(seg * 40 + ct0) * 32768 + lane * 16;
  const char* pp = ws + OFF_PT + (size_t)seg * 32 * 3072;
  const char* vp = ws + OFF_VW + (size_t)seg * VW_STEP + (size_t)ct0 * 8192 + c * 16 + (g & 1) * 8;
  f32x4 acc[2][2];
#pragma unroll
  for (int i = 0; i < 4; ++i) acc[i >> 1][i & 1] = f32x4{0.f, 0.f, 0.f, 0.f};
#pragma unroll 4
  for (int hp = 0; hp < 16; ++hp) {
    u32x4 u[2][2], p[2][2]; u32x2 pm[2][2], vw[2][2];
#pragma unroll
    for (int hh = 0; hh < 2; ++hh) {
      const int h = 2 * hp + hh;
#pragma unroll
      for (int i = 0; i < 2; ++i) {
        u[hh][i] = *(const u32x4*)(up + i * 32768 + h * 1024);
        p[hh][i] = *(const u32x4*)(pp + h * 3072 + i * 1024 + lane * 16);
        pm[hh][i] = *(const u32x2*)(pp + h * 3072 + 2048 + i * 512 + lane * 8);
        vw[hh][i] = *(const u32x2*)(vp + i * 8192 + h * 256);
      }
    }
#pragma unroll
    for (int ci = 0; ci < 2; ++ci)
#pragma unroll
      for (int qt = 0; qt < 2; ++qt) {
        acc[ci][qt] = MFMA16(asb8(u[0][ci]), asb8(p[0][qt]), acc[ci][qt]);
        acc[ci][qt] = MFMA16(asb8(u[1][ci]), asb8(p[1][qt]), acc[ci][qt]);
        u32x4 a, bb;
        a[0] = vw[0][ci][0]; a[1] = vw[0][ci][1]; a[2] = vw[1][ci][0]; a[3] = vw[1][ci][1];
        bb[0] = pm[0][qt][0]; bb[1] = pm[0][qt][1]; bb[2] = pm[1][qt][0]; bb[3] = pm[1][qt][1];
        acc[ci][qt] = MFMA16(asb8(a), asb8(bb), acc[ci][qt]);
      }
  }
  u16* H = (u16*)(ws + OFF_H);
#pragma unroll
  for (int qt = 0; qt < 2; ++qt) {
    const f32x4 a = acc[0][qt], bq = acc[1][qt];
    const u32x4 o = widen(cvtpk(a[0], a[1]), cvtpk(a[2], a[3]), cvtpk(bq[0], bq[1]), cvtpk(bq[2], bq[3]));
    const size_t off = (size_t)(seg * 32 + qt * 16 + c) * D_ + ct0 * 16 + 16 * (g & 1) + 8 * (g >> 1);
    const u32x4 r = *(const u32x4*)(x + off);
    u32x4 z;
#pragma unroll
    for (int i = 0; i < 4; ++i) z[i] = cvtpk(bflo(r[i]) + bflo(o[i]), bfhi(r[i]) + bfhi(o[i]));
    *(u32x4*)(H + off) = z;
  }
}

extern "C" void kernel_launch(void* const* d_in, const int* in_sizes, int n_in, void* d_out, int out_size, void* d_ws, size_t ws_size,
                              hipStream_t stream) {
  const u16* x = (const u16*)d_in[0];
  const u16* mem0 = (const u16*)d_in[1];
  const u16* Wq = (const u16*)d_in[2];
  const u16* Wk = (const u16*)d_in[3];
  const u16* Wv = (const u16*)d_in[4];
  const u16* Wo = (const u16*)d_in[5];
  const u16* Wg = (const u16*)d_in[8];
  const u16* Wu = (const u16*)d_in[9];
  const u16* Wd = (const u16*)d_in[10];
  char* ws = (char*)d_ws;
  u16* XN = (u16*)(ws + OFF_XN);
  prep_kernel<<<512 + FILL_BLOCKS + 1, 256, 0, stream>>>((const unsigned*)x, mem0, ws);
  gemm_kernel<0, 128><<<16 * 64, 256, 0, stream>>>(XN, Wq, Wk, (u16*)(ws + OFF_Q), (u16*)(ws + OFF_K), XN);
  gemmVU_kernel<<<16 * 32, 256, 0, stream>>>(XN, Wv, Wo, ws + OFF_U);
  chain_kernel<<<104, 256, 0, stream>>>(Wq, Wk, Wv, Wo, ws);
  tokP_kernel<<<64 * 8, 256, 0, stream>>>(ws);
  tokO_kernel<<<64 * 5, 256, 0, stream>>>(x, ws);
  ln2_kernel<<<512, 256, 0, stream>>>(ws);
  gemm_kernel<1, 128><<<16 * 27, 256, 0, stream>>>((const u16*)(ws + OFF_HN), Wg, Wu, (u16*)(ws + OFF_ACT), (u16*)(ws + OFF_ACT), XN);
  gemm_kernel<2, 64><<<32 * 5, 256, 0, stream>>>((const u16*)(ws + OFF_ACT), Wd, Wd, (u16*)d_out, (u16*)d_out, (const u16*)(ws + OFF_H));
}
```

```cpp
#include <hip/hip_runtime.h>
#include <hip/hip_bf16.h>
#include <stdint.h>

typedef __bf16 bf16x8 __attribute__((ext_vector_type(8)));
typedef __bf16 bf16x16 __attribute__((ext_vector_type(16)));
typedef __bf16 bf16x4 __attribute__((ext_vector_type(4)));
typedef float f32x4 __attribute__((ext_vector_type(4)));
typedef unsigned u32x4 __attribute__((ext_vector_type(4)));
typedef unsigned u32x2 __attribute__((ext_vector_type(2)));
typedef short s16x4 __attribute__((ext_vector_type(4)));
typedef __attribute__((address_space(3))) char* lds_ptr;
typedef unsigned short u16;

#define DEVI __device__ __forceinline__

constexpr int S_ = 2048, D_ = 640, HE = 4096, F_ = 1728, NSEG = 64;
constexpr int NPRE = 8;
constexpr int NFILL = 60;
constexpr unsigned XN_SEG = 40960, QK_SEG = 32 * 16384, U_SEG = 40 * 32 * 1024;
constexpr unsigned OFF_XN = 0;
constexpr unsigned OFF_QK = OFF_XN + NSEG * XN_SEG;
constexpr unsigned OFF_U = OFF_QK + NSEG * QK_SEG;
constexpr unsigned OFF_FLG = OFF_U + NSEG * U_SEG;
constexpr unsigned OFF_R = OFF_FLG + 72 * 256;
#define RM 8
#define RP 8
constexpr unsigned R_MEM = 0, R_P = RM * 10240, R_VW = R_P + RP * 20480, R_PT = R_VW + 327680, R_H = R_PT + 32 * 2560, R_ACT = R_H + 40960,
                   REC = R_ACT + 54 * 2048;
constexpr unsigned OFF_END = OFF_R + NSEG * REC;
static_assert(OFF_R % 256 == 0 && REC % 256 == 0 && OFF_U % 256 == 0, "align");
static_assert((size_t)OFF_END + (4u << 20) <= (size_t)256 * 1024 * 1024, "ws");

typedef __bf16 bf16x2 __attribute__((ext_vector_type(2)));
typedef float f32x2 __attribute__((ext_vector_type(2)));
DEVI unsigned cvtpk(float lo, float hi) { const f32x2 v = {lo, hi}; return __builtin_bit_cast(unsigned, __builtin_convertvector(v, bf16x2)); }
DEVI float bflo(unsigned u) { return __uint_as_float(u << 16); }
DEVI float bfhi(unsigned u) { return __uint_as_float(u & 0xffff0000u); }
DEVI float rbf(float x) { return bflo(cvtpk(x, x)); }
DEVI unsigned nosent(unsigned u) { return u < 0xFFFFFFFEu ? u : 0xFFFFFFFEu; }
DEVI void glds16(const void* g, unsigned lds_base) {
  unsigned sv;
  asm volatile("s_mov_b32 %0, m0\n\ts_mov_b32 m0, %2\n\ts_nop 0\n\tglobal_load_lds_dwordx4 %1, off\n\ts_mov_b32 m0, %0" : "=&s"(sv) : "v"(g), "s"(lds_base) : "memory");
}
DEVI s16x4 trread(unsigned a) { return __builtin_amdgcn_ds_read_tr16_b64_v4i16((__attribute__((address_space(3))) s16x4*)(uintptr_t)a); }
DEVI bf16x8 cat8(s16x4 a, s16x4 b) { return __builtin_bit_cast(bf16x8, __builtin_shufflevector(a, b, 0, 1, 2, 3, 4, 5, 6, 7)); }
DEVI u32x4 ldsr128(unsigned a) { return *(__attribute__((address_space(3))) u32x4*)(uintptr_t)a; }
DEVI u32x4 ldsr128o(unsigned a) { return *(volatile __attribute__((address_space(3))) u32x4*)(uintptr_t)a; }
DEVI u32x2 ldsr64(unsigned a) { return *(__attribute__((address_space(3))) u32x2*)(uintptr_t)a; }
DEVI void ldsw128(unsigned a, u32x4 v) { *(__attribute__((address_space(3))) u32x4*)(uintptr_t)a = v; }
DEVI void ldsw64(unsigned a, u32x2 v) { *(__attribute__((address_space(3))) u32x2*)(uintptr_t)a = v; }
DEVI bf16x8 asb8(u32x4 v) { return __builtin_bit_cast(bf16x8, v); }
#define MFMA16(a, b, c) __builtin_amdgcn_mfma_f32_16x16x32_bf16(a, b, c, 0, 0, 0)
#define SMF(a, b, c, idx) __builtin_amdgcn_smfmac_f32_16x16x64_bf16(a, b, c, idx, 0, 0)
#define BAR_ALL() asm volatile("s_waitcnt vmcnt(0) lgkmcnt(0)\n\ts_barrier" ::: "memory")
#define BAR_LDS() asm volatile("s_waitcnt lgkmcnt(0)\n\ts_barrier" ::: "memory")
DEVI float xmax16(float x) { auto r = __builtin_amdgcn_permlane16_swap(__float_as_uint(x), __float_as_uint(x), false, false); return fmaxf(__uint_as_float(r[0]), __uint_as_float(r[1])); }
DEVI float xmax32(float x) { auto r = __builtin_amdgcn_permlane32_swap(__float_as_uint(x), __float_as_uint(x), false, false); return fmaxf(__uint_as_float(r[0]), __uint_as_float(r[1])); }
DEVI float xsum16(float x) { auto r = __builtin_amdgcn_permlane16_swap(__float_as_uint(x), __float_as_uint(x), false, false); return __uint_as_float(r[0]) + __uint_as_float(r[1]); }
DEVI float xsum32(float x) { auto r = __builtin_amdgcn_permlane32_swap(__float_as_uint(x), __float_as_uint(x), false, false); return __uint_as_float(r[0]) + __uint_as_float(r[1]); }
DEVI float pairsum32(float a, float b) { auto r = __builtin_amdgcn_permlane32_swap(__float_as_uint(a), __float_as_uint(b), false, false); return __uint_as_float(r[0]) + __uint_as_float(r[1]); }
DEVI void st_sc1_x4(void* p, u32x4 v) { asm volatile("global_store_dwordx4 %0, %1, off sc1\n\ts_nop 1" ::"v"(p), "v"(v) : "memory"); }
DEVI void st_sc1_x2(void* p, u32x2 v) { asm volatile("global_store_dwordx2 %0, %1, off sc1\n\ts_nop 1" ::"v"(p), "v"(v) : "memory"); }
DEVI void st_sc1_x1(void* p, unsigned v) { asm volatile("global_store_dword %0, %1, off sc1\n\ts_nop 1" ::"v"(p), "v"(v) : "memory"); }
DEVI unsigned umax4(u32x4 v) { return max(max(v[0], v[1]), max(v[2], v[3])); }

typedef unsigned long long u64;
typedef __amdgpu_buffer_rsrc_t rsrc_t;
DEVI u32x4 ld16(rsrc_t rs, unsigned v, unsigned s) { return __builtin_amdgcn_raw_buffer_load_b128(rs, v, s, 16); }
DEVI unsigned ld4(rsrc_t rs, unsigned v, unsigned s) { return __builtin_amdgcn_raw_buffer_load_b32(rs, v, s, 16); }
DEVI void st16(rsrc_t rs, unsigned v, unsigned s, u32x4 d) { __builtin_amdgcn_raw_buffer_store_b128(d, rs, v, s, 16); }
DEVI void st8(rsrc_t rs, unsigned v, unsigned s, u32x2 d) { __builtin_amdgcn_raw_buffer_store_b64(d, rs, v, s, 16); }
DEVI void st4(rsrc_t rs, unsigned v, unsigned s, unsigned d) { __builtin_amdgcn_raw_buffer_store_b32(d, rs, v, s, 16); }
#define SB() __builtin_amdgcn_sched_barrier(0)
DEVI void touch(rsrc_t rs, unsigned v, unsigned s) { const unsigned x = ld4(rs, v, s); asm volatile("" :: "v"(x)); }
DEVI unsigned ror8(unsigned x) { return __builtin_amdgcn_mov_dpp(x, 0x128, 0xF, 0xF, true); }
DEVI float xmax8(float x) { return fmaxf(x, __uint_as_float(ror8(__float_as_uint(x)))); }
DEVI float xsum8(float x) { return x + __uint_as_float(ror8(__float_as_uint(x))); }
#define RED8(OP, x, y) do { float t_; asm volatile(OP " %0, %0, %2\n\ts_nop 1\n\t" OP "_dpp %0, %0, %0 row_ror:8 row_mask:0xf bank_mask:0xf\n\tv_mov_b32 %1, %0\n\ts_nop 1\n\t" \
    "v_permlane16_swap_b32 %0, %1\n\t" OP " %0, %0, %1\n\tv_mov_b32 %1, %0\n\ts_nop 1\n\tv_permlane32_swap_b32 %0, %1\n\t" OP " %0, %0, %1" : "+v"(x), "=&v"(t_) : "v"(y)); } while (0)
DEVI float vmax3(float a, float b, float c) { float r; asm("v_max3_f32 %0, %1, %2, %3" : "=v"(r) : "v"(a), "v"(b), "v"(c)); return r; }
DEVI f32x4 pairsum4(f32x4 a, f32x4 b) {
  u32x4 x, y;
#pragma unroll
  for (int i = 0; i < 4; ++i) { auto r = __builtin_amdgcn_permlane32_swap(__float_as_uint(a[i]), __float_as_uint(b[i]), false, false); x[i] = r[0]; y[i] = r[1]; }
  return __builtin_bit_cast(f32x4, x) + __builtin_bit_cast(f32x4, y);
}
#define NOMOVE() asm volatile("" ::: "memory")
#define PENDING(v) __builtin_amdgcn_ballot_w64(umax4(v) == ~0u)
DEVI float xsum2(float x) { return x + __uint_as_float(__builtin_amdgcn_update_dpp(0u, __float_as_uint(x), 0x4E, 0xF, 0xF, true)); }
DEVI float xsum1(float x) { return x + __uint_as_float(__builtin_amdgcn_update_dpp(0u, __float_as_uint(x), 0xB1, 0xF, 0xF, true)); }
DEVI u32x4 widen(unsigned a0, unsigned a1, unsigned b0, unsigned b1) {
  auto r0 = __builtin_amdgcn_permlane16_swap(a0, b0, false, false);
  auto r1 = __builtin_amdgcn_permlane16_swap(a1, b1, false, false);
  u32x4 o; o[0] = r0[0]; o[1] = r1[0]; o[2] = r0[1]; o[3] = r1[1]; return o;
}
DEVI u32x4 widen4(f32x4 x, f32x4 y) { return widen(cvtpk(x[0], x[1]), cvtpk(x[2], x[3]), cvtpk(y[0], y[1]), cvtpk(y[2], y[3])); }
DEVI u32x4 nosent4(u32x4 v) { u32x4 o; o[0] = nosent(v[0]); o[1] = nosent(v[1]); o[2] = nosent(v[2]); o[3] = nosent(v[3]); return o; }

template <int N, class RED>
DEVI void ln_apply(u32x4 (&v)[N], RED red) {
  float s = 0.f;
#pragma unroll
  for (int i = 0; i < N; ++i)
#pragma unroll
    for (int j = 0; j < 4; ++j) s += bflo(v[i][j]) + bfhi(v[i][j]);
  const float mean = red(s) * (1.0f / 640.0f);
  float q = 0.f;
#pragma unroll
  for (int i = 0; i < N; ++i)
#pragma unroll
    for (int j = 0; j < 4; ++j) { const float a = bflo(v[i][j]) - mean, b = bfhi(v[i][j]) - mean; q += a * a + b * b; }
  const float mb = rbf(mean), vb = rbf(red(q) * (1.0f / 640.0f));
  const float sd = rbf(sqrtf(rbf(vb + 1e-05f)));
#pragma unroll
  for (int i = 0; i < N; ++i)
#pragma unroll
    for (int j = 0; j < 4; ++j) v[i][j] = cvtpk(rbf(bflo(v[i][j]) - mb) / sd, rbf(bfhi(v[i][j]) - mb) / sd);
}

template <int N, class RED>
DEVI void ln_fast(u32x4 (&v)[N], RED red) {
  f32x2 s2 = {0.f, 0.f};
#pragma unroll
  for (int i = 0; i < N; ++i)
#pragma unroll
    for (int j = 0; j < 4; ++j) s2 += f32x2{bflo(v[i][j]), bfhi(v[i][j])};
  const float mean = red(s2[0] + s2[1]) * (1.0f / 640.0f);
  const float mb = rbf(mean);
  const f32x2 m2 = {mb, mb};
  f32x2 q2 = {0.f, 0.f};
#pragma unroll
  for (int i = 0; i < N; ++i)
#pragma unroll
    for (int j = 0; j < 4; ++j) { const f32x2 d = f32x2{bflo(v[i][j]), bfhi(v[i][j])} - m2; q2 += d * d; v[i][j] = cvtpk(d[0], d[1]); }
  const float dm = mean - mb;
  const float vb = rbf(fmaxf(red(q2[0] + q2[1]) * (1.0f / 640.0f) - dm * dm, 0.f));
  const float r = 1.0f / rbf(sqrtf(rbf(vb + 1e-05f)));
  const f32x2 r2 = {r, r};
#pragma unroll
  for (int i = 0; i < N; ++i)
#pragma unroll
    for (int j = 0; j < 4; ++j) { const f32x2 d = f32x2{bflo(v[i][j]), bfhi(v[i][j])} * r2; v[i][j] = cvtpk(d[0], d[1]); }
}

constexpr int FILL_BLOCKS = 256;
__global__ __launch_bounds__(256) void prep_kernel(const u16* __restrict__ x, const u16* __restrict__ mem0, char* __restrict__ ws) {
  const int b = blockIdx.x, t = threadIdx.x;
  if (b < 128) {
    const int row = b * 16 + (t >> 4), i = t & 15;
    u32x4 v[5];
#pragma unroll
    for (int j = 0; j < 5; ++j) v[j] = *(const u32x4*)(x + (size_t)row * D_ + (i + 16 * j) * 8);
    ln_apply<5>(v, [](float s) { s += __shfl_xor(s, 1); s += __shfl_xor(s, 2); s += __shfl_xor(s, 4); s += __shfl_xor(s, 8); return s; });
    char* dst = ws + OFF_XN + (size_t)(row >> 5) * XN_SEG + ((row >> 4) & 1) * 1024 + ((i & 3) * 16 + (row & 15)) * 16;
#pragma unroll
    for (int j = 0; j < 5; ++j) *(u32x4*)(dst + (4 * j + (i >> 2)) * 2048) = v[j];
  } else if (b < 128 + FILL_BLOCKS) {
    u32x4* p = (u32x4*)(ws + OFF_R + R_P);
    const unsigned n = (NPRE * REC - R_P) / 16;
    const u32x4 s = {~0u, ~0u, ~0u, ~0u};
    for (unsigned i = (b - 128) * 256 + t; i < n; i += FILL_BLOCKS * 256) p[i] = s;
    if ((b - 128) * 256 + t < QK_SEG / 16) ((u32x4*)(ws + OFF_QK))[(b - 128) * 256 + t] = s;
  } else {
    if (t < 72) ((unsigned*)(ws + OFF_FLG))[t * 64] = 0u;
    for (int i = t; i < 640; i += 256) {
      const int ct = i >> 4, bb = (i >> 3) & 1, r = i & 7;
      const u32x4 m = nosent4(*(const u32x4*)(mem0 + r * 640 + ct * 16 + bb * 8));
#pragma unroll
      for (int j = 0; j < RM; ++j) *(u32x4*)(ws + OFF_R + R_MEM + j * 10240 + (size_t)i * 16) = m;
    }
  }
}

constexpr float SCALE = 0.08838834764831845f, LOG2E = 1.4426950408889634f;
constexpr unsigned SPIN_BUDGET = 1u << 22;
constexpr int CH_STG = 65536;
constexpr int CH_MEM = 0, CH_X = CH_MEM + 10240, CH_LDS = 2 * CH_STG;

#define TS(i)
#define TSW(i)
#define TSDECL
#define TSFLUSH(s)
#define PCHK 3
#define PSLP 1
#define PCAP 6
#define PCAPC 49
#define STR2(x) #x
#define STR(x) STR2(x)
#define CLOB3 "v244", "v245", "v246", "v247", "v248", "v249", "v250", "v251", "v252", "v253", "v254", "v255"
#define CLOB7 "v220", "v221", "v222", "v223", "v224", "v225", "v226", "v227", "v228", "v229", "v230", "v231", "v232", "v233", "v234", "v235", "v236", "v237", "v238", "v239", "v240", "v241", "v242", "v243", CLOB3
#define PEND_ASM(t, m, r0, r1, r2, r3) "v_max_u32 " t ", v" #r2 ", v" #r3 "\n\tv_max3_u32 " t ", v" #r0 ", v" #r1 ", " t "\n\tv_cmp_eq_u32 " m ", -1, " t "\n\t"
#define CHK3 "s_sleep " STR(PSLP) "\n\t" PEND_ASM("%[t]", "%[m0]", 244, 245, 246, 247) PEND_ASM("%[t]", "%[m1]", 248, 249, 250, 251) \
             PEND_ASM("%[t]", "%[m2]", 252, 253, 254, 255) "s_or_b64 %[x], %[m0], %[m1]\n\ts_or_b64 %[x], %[x], %[m2]\n\ts_cbranch_scc0 .Lpd%=\n\t"
#define CHK3N CHK3 CHK3 CHK3
DEVI void poll_mem(const char* memp, unsigned lmem, int w, int lane, unsigned& budget) {
  const char* p0 = memp + w * 2560 + lane * 16;
  const unsigned la = lmem + w * 2560 + lane * 16;
  u64 m0, m1, m2, x, sv; unsigned t, bud = __builtin_amdgcn_readfirstlane(budget);
  asm volatile("s_mov_b64 %[sv], exec\n\ts_mov_b64 %[m0], -1\n\ts_mov_b64 %[m1], -1\n\ts_mov_b64 %[m2], 0xffffffff\n"
               ".Lpl%=:\n\ts_waitcnt vmcnt(" STR(PCAP) ")\n\t"
               "s_mov_b64 exec, %[m0]\n\tglobal_load_dwordx4 v[244:247], %[p0], off sc1\n\t"
               "s_mov_b64 exec, %[m1]\n\tglobal_load_dwordx4 v[248:251], %[p0], off offset:1024 sc1\n\t"
               "s_mov_b64 exec, %[m2]\n\tglobal_load_dwordx4 v[252:255], %[p0], off offset:2048 sc1\n\t"
               "s_mov_b64 exec, %[sv]\n\t"
               CHK3N
               "s_sub_u32 %[bud], %[bud], 1\n\ts_cbranch_scc0 .Lpl%=\n\ts_mov_b32 %[bud], 0\n"
               ".Lpd%=:\n\t"
               "ds_write_b128 %[la], v[244:247]\n\tds_write_b128 %[la], v[248:251] offset:1024\n\t"
               "s_mov_b64 exec, 0xffffffff\n\tds_write_b128 %[la], v[252:255] offset:2048\n\ts_mov_b64 exec, %[sv]\n\t"
               "s_waitcnt lgkmcnt(0)\n\ts_barrier"
               : [m0] "=&s"(m0), [m1] "=&s"(m1), [m2] "=&s"(m2), [x] "=&s"(x), [sv] "=&s"(sv), [t] "=&v"(t), [bud] "+s"(bud)
               : [p0] "v"(p0), [la] "v"(la) : "memory", "scc", CLOB3);
  budget = bud;
}
#define VM0() __builtin_amdgcn_s_waitcnt(0x0F70)
DEVI void drain_mem(int lane) {
  const unsigned z = lane < 32 ? ~0u : 0u;
  VM0();
  asm volatile("s_waitcnt vmcnt(0)\n\tv_mov_b32 v244, -1\n\tv_mov_b32 v245, -1\n\tv_mov_b32 v246, -1\n\tv_mov_b32 v247, -1\n\t"
               "v_mov_b32 v248, -1\n\tv_mov_b32 v249, -1\n\tv_mov_b32 v250, -1\n\tv_mov_b32 v251, -1\n\t"
               "v_mov_b32 v252, %0\n\tv_mov_b32 v253, %0\n\tv_mov_b32 v254, %0\n\tv_mov_b32 v255, %0" :: "v"(z) : "memory", CLOB3);
}
DEVI void wait_prog(rsrc_t rs, unsigned voff, unsigned need, unsigned& seen, unsigned& budget) {
  while (__builtin_amdgcn_ballot_w64(seen < need) != 0 && budget != 0) {
    NOMOVE();
    seen = ld4(rs, voff, OFF_FLG);
    --budget;
    if (__builtin_amdgcn_ballot_w64(seen < need) != 0) __builtin_amdgcn_s_sleep(8);
  }
  NOMOVE();
}
DEVI void wait_fill(rsrc_t rs, int s, unsigned& budget) {
  if ((s & (NPRE - 1)) == NPRE - 3 && s + NPRE < NSEG) { unsigned seen = 0; wait_prog(rs, (64 + s / NPRE) * 256, NFILL, seen, budget); }
}
DEVI void wait_word(rsrc_t rs, unsigned soff, unsigned& budget) {
  for (;;) {
    NOMOVE();
    const unsigned v = ld4(rs, 0, soff);
    if (__builtin_amdgcn_readfirstlane(v) != ~0u || budget == 0) break;
    --budget;
    __builtin_amdgcn_s_sleep(2);
  }
  NOMOVE();
}
template <int N, class OFFV, class OFFS>
DEVI void poll_n(rsrc_t rs, u32x4 (&v)[N], OFFV offv, OFFS offs, unsigned& budget, unsigned pend = (1u << N) - 1u) {
  for (;;) {
    NOMOVE();
#pragma unroll
    for (int i = 0; i < N; ++i) if (pend & (1u << i)) v[i] = ld16(rs, offv(i), offs(i));
    unsigned np = 0;
#pragma unroll
    for (int i = 0; i < N; ++i) np |= (PENDING(v[i]) != 0 ? 1u : 0u) << i;
    pend = np;
    if (pend == 0 || budget == 0) break;
    --budget;
  }
  NOMOVE();
}
template <bool AG>
DEVI void smf2(f32x4& c0, f32x4& c1, bf16x8 a, const bf16x16& w0, const bf16x16& w1, int idx) {
  if (AG) asm volatile("v_smfmac_f32_16x16x64_bf16 %0, %2, %3, %5\n\tv_smfmac_f32_16x16x64_bf16 %1, %2, %4, %5" : "+v"(c0), "+v"(c1) : "v"(a), "a"(w0), "a"(w1), "v"(idx));
  else asm volatile("v_smfmac_f32_16x16x64_bf16 %0, %2, %3, %5\n\tv_smfmac_f32_16x16x64_bf16 %1, %2, %4, %5" : "+v"(c0), "+v"(c1) : "v"(a), "v"(w0), "v"(w1), "v"(idx));
}
template <bool AG, bool WA, bool FIRST>
DEVI void mf2(f32x4& c0, f32x4& c1, const bf16x8& w0, const bf16x8& w1, bf16x8 x) {
#define MF2_ASM(T, CO, WC) asm volatile(T : CO(c0), CO(c1) : WC(w0), WC(w1), "v"(x))
#define T_WA_F "v_mfma_f32_16x16x32_bf16 %0, %2, %4, 0\n\tv_mfma_f32_16x16x32_bf16 %1, %3, %4, 0"
#define T_WA_N "v_mfma_f32_16x16x32_bf16 %0, %2, %4, %0\n\tv_mfma_f32_16x16x32_bf16 %1, %3, %4, %1"
#define T_WB_F "v_mfma_f32_16x16x32_bf16 %0, %4, %2, 0\n\tv_mfma_f32_16x16x32_bf16 %1, %4, %3, 0"
#define T_WB_N "v_mfma_f32_16x16x32_bf16 %0, %4, %2, %0\n\tv_mfma_f32_16x16x32_bf16 %1, %4, %3, %1"
  if (AG) {
    if (WA) { if (FIRST) MF2_ASM(T_WA_F, "=&v", "a"); else MF2_ASM(T_WA_N, "+v", "a"); }
    else { if (FIRST) MF2_ASM(T_WB_F, "=&v", "a"); else MF2_ASM(T_WB_N, "+v", "a"); }
  } else {
    if (WA) { if (FIRST) MF2_ASM(T_WA_F, "=&v", "v"); else MF2_ASM(T_WA_N, "+v", "v"); }
    else { if (FIRST) MF2_ASM(T_WB_F, "=&v", "v"); else MF2_ASM(T_WB_N, "+v", "v"); }
  }
}
template <bool FIRST>
DEVI void mf1(f32x4& c, u32x4 a, bf16x8 b) {
  if (FIRST) asm volatile("v_mfma_f32_16x16x32_bf16 %0, %1, %2, 0" : "=&v"(c) : "v"(a), "v"(b));
  else asm volatile("v_mfma_f32_16x16x32_bf16 %0, %1, %2, %0" : "+v"(c) : "v"(a), "v"(b));
}
#define DLY_AB 0
#define DLY_GD 0
#define QK0
#define TOUCH
#define DLY_CU 60
#define SWG 40
#define SWD 48
#define LASTW 7
#define DA 170
#define DB 195
#define DC 200
#define PNOM 320
struct Pace {
  unsigned last, per, until;
  DEVI void mark(int d) {
    const unsigned now = (unsigned)__builtin_amdgcn_s_memrealtime(), p = now - last;
    until = now + (unsigned)max(d + min((int)min(min(p, per), 1000u) - PNOM, 0), 0);
    last = now; per = p;
  }
  DEVI void pause() const { while ((int)((unsigned)__builtin_amdgcn_s_memrealtime() - until) < 0) __builtin_amdgcn_s_sleep(1); }
};
DEVI void delay_ticks(unsigned n) { const u64 t = __builtin_amdgcn_s_memrealtime() + n; while (__builtin_amdgcn_s_memrealtime() < t) __builtin_amdgcn_s_sleep(4); }
#define NOPS2(n, c0, c1) asm volatile("s_nop " #n : "+v"(c0), "+v"(c1))
#define NOPS4(n, c0, c1, c2, c3) asm volatile("s_nop " #n : "+v"(c0), "+v"(c1), "+v"(c2), "+v"(c3))
constexpr int NVS = 5;

template <bool SP> DEVI int swz(int row) { return SP ? (((row & 1) << 1) | (row & 12)) : (((row & 3) << 2) | ((row >> 2) & 3)); }
template <int NSL, bool SP, int NMID, class SRC, class PUT, class MID>
DEVI void load_slabs(unsigned l0, int w, int lane, int rbase, int rstep, int col0, int colt, SRC src, PUT put, MID mid) {
  constexpr int NB = (NSL + 3) / 4;
  auto stage = [&](int bt) {
#pragma unroll
    for (int j = 0; j < 16; ++j) {
      const int sl = bt * 4 + (j >> 2), blk = w * 4 + (j & 3), pos = blk * 64 + lane;
      if (sl < NSL) glds16(src(sl, pos >> 4, (pos & 15) ^ swz<SP>(pos >> 4)), l0 + (bt & 1) * CH_STG + (j >> 2) * 16384 + blk * 1024);
    }
  };
  stage(0);
#pragma unroll
  for (int bt = 0; bt < NB; ++bt) {
    if (bt + 1 < NB) {
      stage(bt + 1);
      if ((bt + 2) * 4 <= NSL) asm volatile("s_waitcnt vmcnt(16)\n\ts_barrier" ::: "memory");
      else asm volatile("s_waitcnt vmcnt(8)\n\ts_barrier" ::: "memory");
    } else asm volatile("s_waitcnt vmcnt(%0)\n\ts_barrier" :: "n"(NMID) : "memory");
#pragma unroll
    for (int j = 0; j < 4; ++j)
#pragma unroll
      for (int tt = 0; tt < 2; ++tt) {
        if (bt * 4 + j < NSL) {
          s16x4 r[4];
#pragma unroll
          for (int i = 0; i < 4; ++i) {
            const int row = 32 * (i >> 1) + rbase + rstep * (i & 1), cb = (col0 + colt * tt) * 2;
            r[i] = trread(l0 + (bt & 1) * CH_STG + j * 16384 + row * 256 + ((((cb >> 4) ^ swz<SP>(row)) << 4) | (cb & 15)));
          }
          put(bt * 4 + j, tt, cat8(r[0], r[1]), cat8(r[2], r[3]));
        }
      }
    BAR_LDS();
    if (bt + 2 == NB) mid();
  }
}
DEVI const u16* head_src(const u16* p0, const u16* p1, bool qk, int h, int sl, int row, int ch) {
  if (sl < 10) return p0 + (size_t)(sl * 64 + row) * HE + h * 128 + ch * 8;
  const int o = qk ? (sl - 10) * 64 * HE + h * 128 : (h * 128 + ((sl - 10) & 1) * 64) * D_ + ((sl - 10) >> 1) * 128;
  return p1 + o + row * (qk ? HE : D_) + ch * 8;
}
DEVI void fill_share(rsrc_t rs, int s, int idx, int t, char* ws) {
  if ((s & 3) != 0 || (s / 4 + 1) * NPRE >= NSEG) return;
  const int c = s / 4;
  const u32x4 sv = {~0u, ~0u, ~0u, ~0u};
  constexpr unsigned n = NPRE * REC / 4096;
  static_assert(NPRE * REC % 4096 == 0, "fill");
  for (unsigned i = idx; i < n; i += NFILL) st16(rs, t * 16, OFF_R + (c + 1) * NPRE * REC + i * 4096, sv);
  BAR_ALL();
  if (t == 0) __hip_atomic_fetch_add((unsigned*)(ws + OFF_FLG) + (64 + c) * 64, 1u, __ATOMIC_RELAXED, __HIP_MEMORY_SCOPE_AGENT);
}

__global__ __launch_bounds__(256, 1) void mega_kernel(const u16* __restrict__ x, const u16* __restrict__ Wq, const u16* __restrict__ Wk,
                                                       const u16* __restrict__ Wv, const u16* __restrict__ Wo, const u16* __restrict__ Wg,
                                                       const u16* __restrict__ Wu, const u16* __restrict__ Wd, u16* __restrict__ out, char* ws) {
  __shared__ __attribute__((aligned(16))) char smem[CH_LDS];
  const unsigned l0 = (unsigned)(uintptr_t)(lds_ptr)smem;
  const int t = threadIdx.x, lane = t & 63, w = __builtin_amdgcn_readfirstlane(t >> 6);
  const int b = blockIdx.x;
  unsigned budget = SPIN_BUDGET;
  Pace pace = {0u, 0u, 0u};
  const rsrc_t rs = __builtin_amdgcn_make_buffer_rsrc((void*)ws, 0, 0x10000000, 0x00020000);
  char* const rec0 = ws + OFF_R;
  const int g = lane >> 4, c16 = lane & 15, q4 = c16 >> 2, p4 = lane & 3;
  const int gp = 2 * (g & 1) + (g >> 1);
  if (b < 64) {
    const bool roleA = b < 32;
    const int h = b & 31;
    bf16x16 W[20][2];
    if (DLY_AB) delay_ticks(DLY_AB);
    const u16* const p0 = roleA ? Wq : Wv; const u16* const p1 = roleA ? Wk : Wo;
    load_slabs<20, true, 0>(l0, w, lane, 16 * (g >> 1) + 4 * (g & 1) + (q4 & 1) + 8 * (q4 >> 1), 2, 32 * w + 4 * p4, 16,
                   [=](int sl, int row, int ch) { return head_src(p0, p1, roleA, h, sl, row, ch); },
                   [&](int sl, int tt, bf16x8 lo, bf16x8 hi) { W[sl][tt] = __builtin_shufflevector(lo, hi, 0, 1, 2, 3, 4, 5, 6, 7, 8, 9, 10, 11, 12, 13, 14, 15); }, [] {});
    const int sidx = (lane & 8) ? 0xEEEE : 0x4444;
    const unsigned lmem = l0 + CH_MEM, lx = l0 + CH_X;
    drain_mem(lane);

    if (roleA) {
      u32x4 kq[12] = {};
      unsigned seen = 0;
      const unsigned qoff = w == 2 ? 4096 : 0;
      auto fetch_qk = [&](int s) {
        wait_prog(rs, h * 256, s + 1, seen, budget);
        const unsigned so = OFF_QK + (s * 32 + h) * 16384;
#pragma unroll
        for (int i = 0; i < 12; ++i) kq[i] = ld16(rs, lane * 16, so + (i < 8 ? 8192 + i * 1024 : qoff + (i - 8) * 1024));
      };
      const int lim = 16 * (w - 1) + c16;
      const unsigned xw = (32 * w + 16 * (lane >> 5) + c16) * 16 + (g & 1) * 8, xr = (8 * g + q4) * 16 + (p4 & 1) * 8;
      VM0();
      for (int s = 0; s < NSEG; ++s) {
        TSDECL;
        char* const rec = rec0 + (size_t)s * REC;
        f32x4 aq0 = {0.f, 0.f, 0.f, 0.f}, aq1 = aq0, ak0 = aq0, ak1 = aq0;
        NOPS2(0, aq0, aq1); NOPS2(0, ak0, ak1);
        poll_mem(rec + R_MEM + (b % RM) * 10240, lmem, w, lane, budget);
        TS(1);
        bf16x8 af[10];
#pragma unroll
        for (int ks = 0; ks < 10; ++ks) af[ks] = asb8(ldsr128o(lmem + ks * 1024 + lane * 16));
        SB();
#pragma unroll
        for (int ks = 0; ks < 10; ++ks) {
          if (ks < NVS) smf2<false>(aq0, aq1, af[ks], W[ks][0], W[ks][1], sidx); else smf2<true>(aq0, aq1, af[ks], W[ks][0], W[ks][1], sidx);
        }
        smf2<true>(ak0, ak1, af[0], W[10][0], W[10][1], sidx);
        smf2<true>(ak0, ak1, af[1], W[11][0], W[11][1], sidx);
        NOPS2(3, aq0, aq1);
        {
          const f32x4 z = pairsum4(aq0, aq1);
          ldsw64(lx + xw, u32x2{cvtpk(z[0], z[1]), cvtpk(z[2], z[3])});
        }
        BAR_LDS();
        if (__builtin_expect(s == 0 && w < 3, 0)) {
          poll_n<12>(rs, kq, [=](int) { return lane * 16; }, [=](int i) { return OFF_QK + h * 16384 + (i < 8 ? 8192 + i * 1024 : qoff + (i - 8) * 1024); }, budget);
        }
        bf16x8 qb[4];
        if (w == 0) {
#pragma unroll
          for (int ks = 0; ks < 4; ++ks) kq[8 + ks] = __builtin_bit_cast(u32x4, cat8(trread(lx + xr + ks * 512), trread(lx + xr + ks * 512 + 64)));
        }
#pragma unroll
        for (int ks = 0; ks < 4; ++ks) qb[ks] = asb8(kq[8 + ks]);
        SB();
#pragma unroll
        for (int ks = 2; ks < 10; ++ks) smf2<true>(ak0, ak1, af[ks], W[10 + ks][0], W[10 + ks][1], sidx);
        const f32x4 zero = {0.f, 0.f, 0.f, 0.f};
        f32x4 s1, s2;
        mf1<true>(s1, kq[0], qb[0]); mf1<true>(s2, kq[4], qb[0]);
        NOPS2(5, ak0, ak1);
        {
          const f32x4 z = pairsum4(ak0, ak1);
          ldsw64(lx + 2048 + xw, u32x2{cvtpk(z[0], z[1]), cvtpk(z[2], z[3])});
        }
        SB();
        mf1<false>(s1, kq[1], qb[1]); mf1<false>(s2, kq[5], qb[1]); mf1<false>(s1, kq[2], qb[2]);
        TS(2);
        BAR_LDS();
        TS(3);
        bf16x8 xk[4];
#pragma unroll
        for (int ks = 0; ks < 4; ++ks) xk[ks] = cat8(trread(lx + 2048 + xr + ks * 512), trread(lx + 2048 + xr + ks * 512 + 64));
        SB();
        mf1<false>(s2, kq[6], qb[2]); mf1<false>(s1, kq[3], qb[3]); mf1<false>(s2, kq[7], qb[3]);
        NOPS2(7, s1, s2);
        if (w == 0) {
          const bool hi8 = (lane & 8) != 0;
          f32x4 s0;
          const unsigned a = cvtpk(hi8 ? s2[0] : s1[0], hi8 ? s2[1] : s1[1]), c = cvtpk(hi8 ? s2[2] : s1[2], hi8 ? s2[3] : s1[3]);
          SB();
          mf1<true>(s0, __builtin_bit_cast(u32x4, xk[0]), qb[0]);
          const f32x2 sc2 = {SCALE, SCALE}, l2 = {LOG2E, LOG2E};
          const f32x2 a1 = f32x2{bflo(a), bfhi(a)} * sc2, c1 = f32x2{bflo(c), bfhi(c)} * sc2;
          SB();
          mf1<false>(s0, __builtin_bit_cast(u32x4, xk[1]), qb[1]);
          const unsigned a2 = cvtpk(a1[0], a1[1]), c2 = cvtpk(c1[0], c1[1]);
          const f32x2 t01 = {bflo(a2), bfhi(a2)}, t23 = {bflo(c2), bfhi(c2)};
          SB();
          mf1<false>(s0, __builtin_bit_cast(u32x4, xk[2]), qb[2]);
          float mx = vmax3(t01[0], t01[1], t23[0]);
          mx = vmax3(mx, mx, t23[1]);
          SB();
          mf1<false>(s0, __builtin_bit_cast(u32x4, xk[3]), qb[3]);
          f32x2 u01 = t01 * l2, u23 = t23 * l2;
          asm volatile("s_nop 5" : "+v"(s0), "+v"(u01), "+v"(u23));
          const float vm = rbf(rbf(lane >= 32 ? (hi8 ? s0[3] : s0[2]) : (hi8 ? s0[1] : s0[0])) * SCALE);
          RED8("v_max_f32", mx, vm);
          const float ml = mx * LOG2E;
          const f32x2 ml2 = {ml, ml};
          u01 -= ml2; u23 -= ml2;
          const float em = __builtin_amdgcn_exp2f(__builtin_fmaf(vm, LOG2E, -ml));
          const f32x2 e01 = {__builtin_amdgcn_exp2f(u01[0]), __builtin_amdgcn_exp2f(u01[1])}, e23 = {__builtin_amdgcn_exp2f(u23[0]), __builtin_amdgcn_exp2f(u23[1])};
          const f32x2 es = e01 + e23;
          float sum = es[0] + em;
          RED8("v_add_f32", sum, es[1]);
          const float inv = __builtin_amdgcn_rcpf(sum);
          const f32x2 inv2 = {inv, inv}, p01 = e01 * inv2, p23 = e23 * inv2;
          const float pmv = em * inv;
          u32x4 pt;
          pt[0] = nosent(cvtpk(p01[0], p01[1])); pt[1] = nosent(cvtpk(p23[0], p23[1]));
          pt[2] = ror8(pt[0]); pt[3] = ror8(pt[1]);
          const unsigned d0 = nosent(cvtpk(pmv, __uint_as_float(ror8(__float_as_uint(pmv)))));
          const unsigned dm = __builtin_amdgcn_update_dpp(d0, d0, 0x128, 0xF, 0xC, false);
          auto rm = __builtin_amdgcn_permlane32_swap(dm, dm, false, false);
          const unsigned po = OFF_R + s * REC + R_P + h * 640;
#pragma unroll
          for (int j = 0; j < RP / 2; ++j) st16(rs, (c16 >> 3) * 20480 + (c16 & 7) * 64 + g * 16, po + j * 40960, pt);
#pragma unroll
          for (int j = 0; j < RP / 4; ++j) st8(rs, ((c16 >> 3) + (g >> 1) * 2) * 20480 + 512 + (c16 & 7) * 16 + (g & 1) * 8, po + j * 81920, u32x2{rm[0], rm[1]});
          TS(4); pace.mark(DA); TSW(5);
        } else {
          if (w < 3) {
            f32x4 s0 = MFMA16(xk[0], qb[0], zero);
            s0 = MFMA16(xk[1], qb[1], s0); s0 = MFMA16(xk[2], qb[2], s0); s0 = MFMA16(xk[3], qb[3], s0);
            float v[12];
#pragma unroll
            for (int i = 0; i < 4; ++i) {
              v[i] = g < 2 ? rbf(rbf(s0[i]) * SCALE) : -INFINITY;
              v[4 + i] = (4 * g + i <= lim) ? rbf(rbf(s1[i]) * SCALE) : -INFINITY;
              v[8 + i] = (16 + 4 * g + i <= lim) ? rbf(rbf(s2[i]) * SCALE) : -INFINITY;
            }
            float mx = v[0];
#pragma unroll
            for (int i = 1; i < 12; ++i) mx = fmaxf(mx, v[i]);
            mx = xmax32(xmax16(mx));
            float sum = 0.f;
#pragma unroll
            for (int i = 0; i < 12; ++i) { v[i] = __builtin_amdgcn_exp2f((v[i] - mx) * LOG2E); sum += v[i]; }
            sum = xsum32(xsum16(sum));
            const float inv = __builtin_amdgcn_rcpf(sum);
            u32x4 pt; u32x2 pm;
            pm[0] = nosent(cvtpk(v[0] * inv, v[1] * inv)); pm[1] = nosent(cvtpk(v[2] * inv, v[3] * inv));
            pt[0] = nosent(cvtpk(v[4] * inv, v[5] * inv)); pt[1] = nosent(cvtpk(v[6] * inv, v[7] * inv));
            pt[2] = nosent(cvtpk(v[8] * inv, v[9] * inv)); pt[3] = nosent(cvtpk(v[10] * inv, v[11] * inv));
            char* pd = rec + R_PT + h * 2560;
            st_sc1_x4(pd + (w - 1) * 1024 + lane * 16, pt);
            if (g < 2) st_sc1_x2(pd + 2048 + (16 * (w - 1) + c16) * 16 + g * 8, pm);
            TS(4); pace.mark(DA - 15); TSW(5);
          } else pace.mark(DA + 25);
        }
        if (w < 3 && s + 1 < NSEG) fetch_qk(s + 1);
        if (s + 1 < NSEG) touch(rs, w == 0 ? R_P + (lane & 7) * 20480 + h * 640 : R_PT + h * 2560, OFF_R + (s + 1) * REC);
        drain_mem(lane);
        wait_fill(rs, s, budget);
        pace.pause();
        TSFLUSH(s);
      }
    } else {
      for (int s = 0; s < NSEG; ++s) {
        TSDECL;
        char* const rec = rec0 + (size_t)s * REC;
        f32x4 a0 = {0.f, 0.f, 0.f, 0.f}, a1 = a0;
        f32x4 c[5][2];
#pragma unroll
        for (int cb = 0; cb < 5; ++cb) { c[cb][0] = a0; c[cb][1] = a0; NOPS2(0, c[cb][0], c[cb][1]); }
        NOPS2(0, a0, a1);
        poll_mem(rec + R_MEM + (b % RM) * 10240, lmem, w, lane, budget);
        TS(1);
        bf16x8 af[10];
#pragma unroll
        for (int ks = 0; ks < 10; ++ks) af[ks] = asb8(ldsr128o(lmem + ks * 1024 + lane * 16));
        __builtin_amdgcn_sched_barrier(0);
#pragma unroll
        for (int ks = 0; ks < 10; ++ks) {
          if (ks < NVS) smf2<false>(a0, a1, af[ks], W[ks][0], W[ks][1], sidx); else smf2<true>(a0, a1, af[ks], W[ks][0], W[ks][1], sidx);
        }
        NOPS2(7, a0, a1);
        {
          const f32x4 z = pairsum4(a0, a1);
          const u32x2 o = {cvtpk(z[0], z[1]), cvtpk(z[2], z[3])};
          ldsw64(lx + (32 * w + 16 * (lane >> 5) + c16) * 16 + (g & 1) * 8, o);
        }
        TS(2);
        BAR_LDS();
        TS(3);
        bf16x8 va[2];
#pragma unroll
        for (int ks = 0; ks < 2; ++ks) {
          const s16x4 x0 = trread(lx + (64 * ks + 16 * g + 8 * (p4 >> 1) + q4) * 16 + (p4 & 1) * 8);
          const s16x4 x1 = trread(lx + (64 * ks + 16 * g + 8 * (p4 >> 1) + 4 + q4) * 16 + (p4 & 1) * 8);
          va[ks] = cat8(x0, x1);
        }
        char* vd = rec + R_VW + (size_t)(2 * w + (lane >> 5)) * 8192 + h * 256 + c16 * 16 + (g & 1) * 8;
#pragma unroll
        for (int cb = 0; cb < 6; ++cb) {
          if (cb < 5) {
            smf2<true>(c[cb][0], c[cb][1], va[0], W[10 + 2 * cb][0], W[10 + 2 * cb][1], sidx);
            smf2<true>(c[cb][0], c[cb][1], va[1], W[11 + 2 * cb][0], W[11 + 2 * cb][1], sidx);
          }
          if (cb > 0) {
            f32x4 c0 = c[cb - 1][0], c1 = c[cb - 1][1];
            if (cb < 5) NOPS2(3, c0, c1); else NOPS2(7, c0, c1);
            const f32x4 z = pairsum4(c0, c1);
            const u32x2 o = {nosent(cvtpk(z[0], z[1])), nosent(cvtpk(z[2], z[3]))};
            st_sc1_x2(vd + (cb - 1) * 65536, o);
          }
        }
        TS(4); pace.mark(DB); TSW(5);
        if (s + 1 < NSEG) touch(rs, R_VW + (2 * w + (lane >> 5)) * 8192 + h * 256 + min(lane & 7, 4) * 65536, OFF_R + (s + 1) * REC);
        drain_mem(lane);
        wait_fill(rs, s, budget);
        pace.pause();
        TSFLUSH(s);
      }
    }
  } else if (b < 104) {
    const int ct = b - 64;
    u32x4 uf[8];
    unsigned seen = 0;
    auto fetch_u = [&](int s) {
      wait_prog(rs, (32 + 8 * w + (lane & 7)) * 256, s + 1, seen, budget);
#pragma unroll
      for (int i = 0; i < 8; ++i) uf[i] = ld16(rs, lane * 16, OFF_U + ((s * 40 + ct) * 32 + 8 * w + i) * 1024);
    };
    const int r = lane & 7, hb = (lane >> 3) & 1;
#define PCHKC 4
#define PSLPC 1
#define CHK7 "s_sleep " STR(PSLPC) "\n\t" PEND_ASM("%[t]", "%[k5]", 248, 249, 250, 251) PEND_ASM("%[t]", "%[k6]", 252, 253, 254, 255) \
             PEND_ASM("%[t]", "%[k0]", 228, 229, 230, 231) "s_or_b64 %[x], %[k5], %[k6]\n\t" PEND_ASM("%[t]", "%[k1]", 232, 233, 234, 235) "s_or_b64 %[x], %[x], %[k0]\n\t" \
             PEND_ASM("%[t]", "%[k2]", 236, 237, 238, 239) "s_or_b64 %[x], %[x], %[k1]\n\t" PEND_ASM("%[t]", "%[k3]", 240, 241, 242, 243) "s_or_b64 %[x], %[x], %[k2]\n\t" \
             PEND_ASM("%[t]", "%[k4]", 244, 245, 246, 247) "s_or_b64 %[x], %[x], %[k3]\n\ts_or_b64 %[x], %[x], %[k4]\n\ts_cbranch_scc0 .Lcd%=\n\t"
#define CHK7N CHK7 CHK7 CHK7 CHK7
#define NSLOW 2
#define PCHKF 2
#define CHK7F CHK7 CHK7
#define ROUND7 "s_mov_b64 exec, %[k5]\n\tglobal_load_dwordx4 v[248:251], %[vp], off sc1\n\t" \
               "s_mov_b64 exec, %[k6]\n\tglobal_load_dwordx4 v[252:255], %[vp], off offset:1024 sc1\n\t" \
               "s_mov_b64 exec, %[k0]\n\tglobal_load_dwordx4 v[228:231], %[pt], off sc1\n\t" \
               "s_mov_b64 exec, %[k1]\n\tglobal_load_dwordx4 v[232:235], %[pt], off offset:1280 sc1\n\t" \
               "s_mov_b64 exec, %[k2]\n\tglobal_load_dwordx4 v[236:239], %[pt], off offset:2560 sc1\n\t" \
               "s_mov_b64 exec, %[k3]\n\tglobal_load_dwordx4 v[240:243], %[pt], off offset:3840 sc1\n\t" \
               "s_mov_b64 exec, %[k4]\n\tglobal_load_dwordx4 v[244:247], %[pm], off sc1\n\t" \
               "s_mov_b64 exec, %[sv]\n\t"
#define M4(a, b, c, d) "v_mov_b32 v" #a ", -1\n\tv_mov_b32 v" #b ", -1\n\tv_mov_b32 v" #c ", -1\n\tv_mov_b32 v" #d ", -1\n\t"
#define DRAIN7() do { VM0(); asm volatile("s_waitcnt vmcnt(0)\n\t" M4(228, 229, 230, 231) M4(232, 233, 234, 235) M4(236, 237, 238, 239) M4(240, 241, 242, 243) \
                            M4(244, 245, 246, 247) M4(248, 249, 250, 251) M4(252, 253, 254, 255) ::: "memory", CLOB7); } while (0)
    const unsigned rpo = R_P + (ct % RP) * 20480;
    auto swap_u = [&]() {
      if (ct & 1) {
#pragma unroll
        for (int i = 0; i < 8; ++i) uf[i] = u32x4{uf[i][2], uf[i][3], uf[i][0], uf[i][1]};
      }
    };
    fetch_u(0);
    DRAIN7();
    swap_u();
    for (int s = 0; s < NSEG - 1; ++s) {
      char* const rec = rec0 + (size_t)s * REC;
      const unsigned lred = l0 + (s & 1) * 4096;
      const char* pt = rec + rpo + (8 * w + hb) * 640 + r * 64 + g * 16;
      const char* pm = rec + rpo + (8 * w + 4 * hb + g) * 640 + 512 + r * 16;
      const char* vp = rec + R_VW + (size_t)ct * 8192 + 8 * w * 256 + lane * 16;
      u64 k0, k1, k2, k3, k4, k5, k6, x, sv; unsigned tmp, bud = __builtin_amdgcn_readfirstlane(budget);
      unsigned ra = lred + (g * 8 + r) * 16;
      asm volatile("" : "+v"(ra));
      const unsigned wa = ra + hb * 2048 + w * 512;
      TSDECL;
      asm volatile("s_mov_b64 %[sv], exec\n\ts_mov_b64 %[k0], -1\n\ts_mov_b64 %[k1], -1\n\ts_mov_b64 %[k2], -1\n\ts_mov_b64 %[k3], -1\n\t"
                   "s_mov_b64 %[k4], -1\n\ts_mov_b64 %[k5], -1\n\ts_mov_b64 %[k6], -1\n"
                   ROUND7 CHK7N
                   ROUND7 CHK7N
                   ".Lcl%=:\n\ts_waitcnt vmcnt(" STR(PCAPC) ")\n\t"
                   ROUND7 CHK7F
                   "s_sub_u32 %[bud], %[bud], 1\n\ts_cbranch_scc0 .Lcl%=\n\ts_mov_b32 %[bud], 0\n"
                   ".Lcd%=:\n\t"
                   "v_mfma_f32_16x16x32_bf16 v[220:223], %[u0], v[228:231], 0\n\tv_mfma_f32_16x16x32_bf16 v[224:227], %[u1], v[228:231], 0\n\t"
                   "v_mfma_f32_16x16x32_bf16 v[220:223], %[u2], v[232:235], v[220:223]\n\tv_mfma_f32_16x16x32_bf16 v[224:227], %[u3], v[232:235], v[224:227]\n\t"
                   "v_mfma_f32_16x16x32_bf16 v[220:223], %[u4], v[236:239], v[220:223]\n\tv_mfma_f32_16x16x32_bf16 v[224:227], %[u5], v[236:239], v[224:227]\n\t"
                   "v_mfma_f32_16x16x32_bf16 v[220:223], %[u6], v[240:243], v[220:223]\n\tv_mfma_f32_16x16x32_bf16 v[224:227], %[u7], v[240:243], v[224:227]\n\t"
                   "v_mfma_f32_16x16x32_bf16 v[220:223], v[248:251], v[244:247], v[220:223]\n\tv_mfma_f32_16x16x32_bf16 v[224:227], v[252:255], v[244:247], v[224:227]\n\t"
                   "s_nop 7\n\t"
                   "v_add_f32_dpp v220, v224, v220 row_ror:8 row_mask:0xf bank_mask:0xf\n\tv_add_f32_dpp v221, v225, v221 row_ror:8 row_mask:0xf bank_mask:0xf\n\t"
                   "v_add_f32_dpp v222, v226, v222 row_ror:8 row_mask:0xf bank_mask:0xf\n\tv_add_f32_dpp v223, v227, v223 row_ror:8 row_mask:0xf bank_mask:0xf\n\t"
                   "ds_write_b128 %[wa], v[220:223]\n\ts_waitcnt lgkmcnt(0)\n\ts_barrier"
                   : [k0] "=&s"(k0), [k1] "=&s"(k1), [k2] "=&s"(k2), [k3] "=&s"(k3), [k4] "=&s"(k4), [k5] "=&s"(k5), [k6] "=&s"(k6), [x] "=&s"(x),
                     [sv] "=&s"(sv), [t] "=&v"(tmp), [bud] "+s"(bud)
                   : [wa] "v"(wa), [pt] "v"(pt), [pm] "v"(pm), [vp] "v"(vp), [u0] "v"(uf[0]), [u1] "v"(uf[1]), [u2] "v"(uf[2]), [u3] "v"(uf[3]),
                     [u4] "v"(uf[4]), [u5] "v"(uf[5]), [u6] "v"(uf[6]), [u7] "v"(uf[7]) : "memory", "scc", CLOB7);
      budget = bud;
      TS(0);
      TS(2);
      {
        f32x4 z = __builtin_bit_cast(f32x4, ldsr128(ra)) + __builtin_bit_cast(f32x4, ldsr128(ra + 512));
        z += __builtin_bit_cast(f32x4, ldsr128(ra + 1024)) + __builtin_bit_cast(f32x4, ldsr128(ra + 1536));
        const u32x2 o = {nosent(cvtpk(z[0], z[1])), nosent(cvtpk(z[2], z[3]))};
#pragma unroll
        for (int j = 0; j < RM / 8; ++j)
          st8(rs, hb * 10240 + (g >> 1) * 128 + r * 16 + (g & 1) * 8, OFF_R + (s + 1) * REC + R_MEM + (w * (RM / 4) + 2 * j) * 10240 + ct * 256, o);
      }
      TS(3); pace.mark(DC); TSW(4);
      TSFLUSH(s);
      if (s > 1) delay_ticks(DLY_CU);
      if (s + 2 < NSEG) fetch_u(s + 1);
      if (s + 2 < NSEG) touch(rs, R_MEM + (2 * w + hb) * 10240 + ct * 256, OFF_R + (s + 2) * REC);
      DRAIN7();
      swap_u();
      wait_fill(rs, s, budget);
      pace.pause();
    }
  } else if (b < 168) {
    const bool qk = b < 136;
    const int h = (b - 104) & 31;
    bf16x8 W[20][2][2];
    const u16* const p0 = qk ? Wq : Wv; const u16* const p1 = qk ? Wk : Wo;
    auto xbuf = [=](int s) { return l0 + ((s & 1) ? 0u : 2 * XN_SEG); };
    auto stageX = [&](int s) {
#pragma unroll
      for (int j = 0; j < 10; ++j) glds16(ws + OFF_XN + (size_t)s * XN_SEG + (w * 10 + j) * 1024 + lane * 16, xbuf(s) + (w * 10 + j) * 1024);
    };
    load_slabs<20, false, 10>(l0, w, lane, 8 * g + q4, 4, 32 * w + 4 * p4, 16,
                   [=](int sl, int row, int ch) { return head_src(p0, p1, qk, h, sl, row, ch); },
                   [&](int sl, int tt, bf16x8 lo, bf16x8 hi) { W[sl][tt][0] = lo; W[sl][tt][1] = hi; }, [&] { stageX(0); });
    const unsigned lv = l0 + XN_SEG;
    const unsigned pword = OFF_FLG + (b - 104) * 256;
    for (int s = 0; s < NSEG; ++s) {
      TSDECL;
      if (s <= 4) asm volatile("s_waitcnt vmcnt(0)" ::: "memory");
      BAR_LDS();
      if (t == 0 && s >= 1) st4(rs, 0, pword, s <= 4 ? s : s - 1);
      if (s + 1 < NSEG) stageX(s + 1);
      TS(0);
      const unsigned lxb = xbuf(s) + lane * 16;
      bf16x8 xa = asb8(ldsr128(lxb)), xb = asb8(ldsr128(lxb + 1024));
      if (qk) {
        f32x4 acc[2][4];
#pragma unroll
        for (int ks = 0; ks < 20; ++ks) {
          bf16x8 na = xa, nb = xb;
          if (ks + 1 < 20) { na = asb8(ldsr128(lxb + (ks + 1) * 2048)); nb = asb8(ldsr128(lxb + (ks + 1) * 2048 + 1024)); }
          __builtin_amdgcn_sched_barrier(0);
          const int sl = ks >> 1, kk = ks & 1;
#define QKSTEP(F) \
          if (sl < NVS) { mf2<false, true, F>(acc[0][0], acc[0][1], W[sl][0][kk], W[sl][1][kk], xa); mf2<false, true, F>(acc[1][0], acc[1][1], W[sl][0][kk], W[sl][1][kk], xb); } \
          else { mf2<true, true, F>(acc[0][0], acc[0][1], W[sl][0][kk], W[sl][1][kk], xa); mf2<true, true, F>(acc[1][0], acc[1][1], W[sl][0][kk], W[sl][1][kk], xb); } \
          mf2<true, true, F>(acc[0][2], acc[0][3], W[10 + sl][0][kk], W[10 + sl][1][kk], xa); mf2<true, true, F>(acc[1][2], acc[1][3], W[10 + sl][0][kk], W[10 + sl][1][kk], xb);
          if (ks == 0) { QKSTEP(true) } else { QKSTEP(false) }
          __builtin_amdgcn_sched_barrier(0);
          xa = na; xb = nb;
        }
        NOPS4(3, acc[0][0], acc[0][1], acc[0][2], acc[0][3]); NOPS4(3, acc[1][0], acc[1][1], acc[1][2], acc[1][3]);
        TS(1);
        asm volatile("s_waitcnt vmcnt(0)" ::: "memory");
        TS(2);
        const unsigned so = OFF_QK + (s * 32 + h) * 16384 + w * 1024, vo = (gp * 16 + c16) * 16;
#pragma unroll
        for (int mt = 0; mt < 2; ++mt) {
          st16(rs, vo, so + mt * 4096, nosent4(widen4(acc[mt][0], acc[mt][1])));
          st16(rs, vo, so + 8192 + mt * 4096, nosent4(widen4(acc[mt][2], acc[mt][3])));
        }
      } else {
        f32x4 av[2][2];
#pragma unroll
        for (int ks = 0; ks < 20; ++ks) {
          bf16x8 na = xa, nb = xb;
          if (ks + 1 < 20) { na = asb8(ldsr128(lxb + (ks + 1) * 2048)); nb = asb8(ldsr128(lxb + (ks + 1) * 2048 + 1024)); }
          __builtin_amdgcn_sched_barrier(0);
          const int sl = ks >> 1, kk = ks & 1;
#define VSTEP(F) \
          if (sl < NVS) { mf2<false, true, F>(av[0][0], av[0][1], W[sl][0][kk], W[sl][1][kk], xa); mf2<false, true, F>(av[1][0], av[1][1], W[sl][0][kk], W[sl][1][kk], xb); } \
          else { mf2<true, true, F>(av[0][0], av[0][1], W[sl][0][kk], W[sl][1][kk], xa); mf2<true, true, F>(av[1][0], av[1][1], W[sl][0][kk], W[sl][1][kk], xb); }
          if (ks == 0) { VSTEP(true) } else { VSTEP(false) }
          __builtin_amdgcn_sched_barrier(0);
          xa = na; xb = nb;
        }
        NOPS4(7, av[0][0], av[0][1], av[1][0], av[1][1]);
        ldsw128(lv + (w * 2) * 1024 + (gp * 16 + c16) * 16, widen4(av[0][0], av[0][1]));
        ldsw128(lv + (w * 2 + 1) * 1024 + (gp * 16 + c16) * 16, widen4(av[1][0], av[1][1]));
        BAR_LDS();
        bf16x8 vf[4][2];
#pragma unroll
        for (int i = 0; i < 8; ++i) vf[i >> 1][i & 1] = asb8(ldsr128(lv + i * 1024 + lane * 16));
        TS(1);
        asm volatile("s_waitcnt vmcnt(0)" ::: "memory");
        TS(2);
        __builtin_amdgcn_sched_barrier(0);
        f32x4 cu[5][2][2];
#pragma unroll
        for (int cb = 0; cb < 6; ++cb) {
          if (cb < 5) {
#pragma unroll
            for (int ks = 0; ks < 4; ++ks) {
              const int sl = 10 + 2 * cb + (ks >> 1), kk = ks & 1;
              if (ks == 0) { mf2<true, false, true>(cu[cb][0][0], cu[cb][0][1], W[sl][0][kk], W[sl][1][kk], vf[ks][0]); mf2<true, false, true>(cu[cb][1][0], cu[cb][1][1], W[sl][0][kk], W[sl][1][kk], vf[ks][1]); }
              else { mf2<true, false, false>(cu[cb][0][0], cu[cb][0][1], W[sl][0][kk], W[sl][1][kk], vf[ks][0]); mf2<true, false, false>(cu[cb][1][0], cu[cb][1][1], W[sl][0][kk], W[sl][1][kk], vf[ks][1]); }
            }
          }
          if (cb > 0) {
            f32x4 x0 = cu[cb - 1][0][0], x1 = cu[cb - 1][0][1], y0 = cu[cb - 1][1][0], y1 = cu[cb - 1][1][1];
            if (cb < 5) NOPS4(0, x0, x1, y0, y1); else NOPS4(7, x0, x1, y0, y1);
            const unsigned so = OFF_U + ((s * 40 + (cb - 1) * 8 + 2 * w) * 32 + h) * 1024;
            u32x4 o;
            o[0] = cvtpk(x0[0], x0[1]); o[1] = cvtpk(x0[2], x0[3]); o[2] = cvtpk(y0[0], y0[1]); o[3] = cvtpk(y0[2], y0[3]);
            st16(rs, lane * 16, so, o);
            o[0] = cvtpk(x1[0], x1[1]); o[1] = cvtpk(x1[2], x1[3]); o[2] = cvtpk(y1[0], y1[1]); o[3] = cvtpk(y1[2], y1[3]);
            st16(rs, lane * 16, so + 32768, o);
          }
        }
      }
      TS(3);
      TSFLUSH(s);
    }
    BAR_ALL();
    if (t == 0) st4(rs, 0, pword, NSEG);
  }
  if (b >= 168 && b < 208) {
    const int ct = b - 168;
    u32x4 uf[8];
    unsigned seen = 0;
    auto fetch_u = [&](int s) {
      wait_prog(rs, (32 + 8 * w + (lane & 7)) * 256, s + 1, seen, budget);
#pragma unroll
      for (int i = 0; i < 8; ++i) uf[i] = ld16(rs, lane * 16, OFF_U + ((s * 40 + ct) * 32 + 8 * w + i) * 1024);
    };
    for (int s = 0; s < NSEG; ++s) {
      TSDECL;
      const unsigned ro = OFF_R + s * REC;
      u32x2 xr = {0u, 0u};
      if (w < 2) xr = *(const u32x2*)(x + (size_t)(s * 32 + 16 * w + c16) * D_ + ct * 16 + 4 * g);
      if (s + 1 < NSEG || (LASTW & 1)) wait_word(rs, ro + R_PT + (8 * w + 7) * 2560 + 1024, budget);
      if (s == 0) fetch_u(0);
      TS(0);
      u32x4 v[22];
      poll_n<22>(rs, v,
                 [=](int i) { return i < 16 ? lane * 16 : (i < 20 ? g * 2560 + c16 * 16 : lane * 16); },
                 [=](int i) { return i < 16 ? ro + R_PT + (8 * w + (i >> 1)) * 2560 + (i & 1) * 1024
                                            : (i < 20 ? ro + R_PT + (8 * w + 4 * ((i - 16) >> 1)) * 2560 + 2048 + (i & 1) * 256
                                                      : ro + R_VW + ct * 8192 + (8 * w + 4 * (i - 20)) * 256); }, budget);
      TS(1);
      f32x4 a0 = {0.f, 0.f, 0.f, 0.f}, a1 = a0;
#pragma unroll
      for (int i = 0; i < 8; ++i) { a0 = MFMA16(asb8(uf[i]), asb8(v[2 * i]), a0); a1 = MFMA16(asb8(uf[i]), asb8(v[2 * i + 1]), a1); }
#pragma unroll
      for (int i = 0; i < 2; ++i) { a0 = MFMA16(asb8(v[20 + i]), asb8(v[16 + 2 * i]), a0); a1 = MFMA16(asb8(v[20 + i]), asb8(v[17 + 2 * i]), a1); }
      const unsigned lred = l0 + (s & 1) * 8192;
      ldsw128(lred + (w * 2) * 1024 + lane * 16, __builtin_bit_cast(u32x4, a0));
      ldsw128(lred + (w * 2 + 1) * 1024 + lane * 16, __builtin_bit_cast(u32x4, a1));
      BAR_LDS();
      if (w < 2) {
        f32x4 z = __builtin_bit_cast(f32x4, ldsr128(lred + w * 1024 + lane * 16));
#pragma unroll
        for (int i = 1; i < 4; ++i) z += __builtin_bit_cast(f32x4, ldsr128(lred + (i * 2 + w) * 1024 + lane * 16));
        u32x2 o;
        o[0] = nosent(cvtpk(bflo(xr[0]) + rbf(z[0]), bfhi(xr[0]) + rbf(z[1])));
        o[1] = nosent(cvtpk(bflo(xr[1]) + rbf(z[2]), bfhi(xr[1]) + rbf(z[3])));
        st8(rs, c16 * 32 + g * 8, ro + R_H + ct * 1024 + w * 512, o);
      }
      TS(2);
      TSFLUSH(s);
      if (s + 1 < NSEG) fetch_u(s + 1);
      fill_share(rs, s, ct, t, ws);
      wait_fill(rs, s, budget);
    }
  } else if ((b >= 208 && b < 235) || (b >= 104 && b < 131)) {
    const int mt2 = b < 208 ? 1 : 0, j = b - (mt2 ? 104 : 208);
    bf16x8 W[20][2];
    load_slabs<10, false, 0>(l0, w, lane, 8 * g + q4, 4, 16 * w + 4 * p4, 64,
                   [=](int sl, int row, int ch) { return Wg + (ch < 8 ? (ptrdiff_t)0 : Wu - Wg) + (size_t)(sl * 64 + row) * F_ + 64 * j + (ch & 7) * 8; },
                   [&](int sl, int tt, bf16x8 lo, bf16x8 hi) { W[2 * sl][tt] = lo; W[2 * sl + 1][tt] = hi; }, [] {});
    const int row = 8 * w + (c16 >> 1), half = lane & 1;
    const unsigned hvo = g * 1024 + row * 32 + half * 16;
    u32x4 v[10];
#pragma unroll
    for (int i = 0; i < 10; ++i) v[i] = u32x4{~0u, ~0u, ~0u, ~0u};
    for (int s = mt2 ? SWG : 0; s < SWG; ++s) {
      TSDECL;
      const unsigned ro = OFF_R + s * REC;
      unsigned pend = 0;
#pragma unroll
      for (int i = 0; i < 10; ++i) pend |= (PENDING(v[i]) != 0 ? 1u : 0u) << i;
      if (pend == 0x3FFu) wait_word(rs, ro + R_H + w * 256, budget);
      TS(0);
      if (pend) poll_n<10>(rs, v, [=](int) { return hvo; }, [=](int i) { return ro + R_H + i * 4096; }, budget, pend);
      TS(1);
      ln_fast<10>(v, [](float z) { return xsum32(xsum16(xsum1(z))); });
      const unsigned lh = l0 + (s & 1) * XN_SEG;
#pragma unroll
      for (int i = 0; i < 10; ++i)
        ldsw128(lh + ((2 * i + (g >> 1)) * 2 + (row >> 4)) * 1024 + ((2 * (g & 1) + half) * 16 + (row & 15)) * 16, v[i]);
      wait_fill(rs, s, budget);
      if (s + 1 < SWG) {
#pragma unroll
        for (int i = 0; i < 10; ++i) v[i] = ld16(rs, hvo, ro + REC + R_H + i * 4096);
      }
      BAR_LDS();
      TS(2);
      f32x4 acc[2][2];
      bf16x8 xf[20][2];
#pragma unroll
      for (int ks = 0; ks < 4; ++ks) { xf[ks][0] = asb8(ldsr128(lh + ks * 2048 + lane * 16)); xf[ks][1] = asb8(ldsr128(lh + ks * 2048 + 1024 + lane * 16)); }
#pragma unroll
      for (int ks = 0; ks < 20; ++ks) {
        if (ks + 4 < 20) { xf[ks + 4][0] = asb8(ldsr128(lh + (ks + 4) * 2048 + lane * 16)); xf[ks + 4][1] = asb8(ldsr128(lh + (ks + 4) * 2048 + 1024 + lane * 16)); }
        __builtin_amdgcn_sched_barrier(0);
        if (ks == 0) { mf2<true, true, true>(acc[0][0], acc[0][1], W[ks][0], W[ks][1], xf[ks][0]); mf2<true, true, true>(acc[1][0], acc[1][1], W[ks][0], W[ks][1], xf[ks][1]); }
        else { mf2<true, true, false>(acc[0][0], acc[0][1], W[ks][0], W[ks][1], xf[ks][0]); mf2<true, true, false>(acc[1][0], acc[1][1], W[ks][0], W[ks][1], xf[ks][1]); }
        __builtin_amdgcn_sched_barrier(0);
      }
      NOPS4(7, acc[0][0], acc[0][1], acc[1][0], acc[1][1]);
      f32x4 a[2];
#pragma unroll
      for (int mt = 0; mt < 2; ++mt)
#pragma unroll
        for (int i = 0; i < 4; ++i) { const float gt = rbf(acc[mt][0][i]), up = rbf(acc[mt][1][i]); a[mt][i] = (gt > 0.f ? gt : gt * 0.f) * up; }
      st16(rs, (g & 1) * 1024 + ((2 * (w & 1) + (g >> 1)) * 16 + c16) * 16, ro + R_ACT + (2 * j + (w >> 1)) * 2048, nosent4(widen4(a[0], a[1])));
      TS(3);
      TSFLUSH(s);
    }
    if (mt2) wait_fill(rs, SWG - 3, budget);
    const int rr = 4 * w + q4;
    const unsigned hvo2 = g * 2048 + (p4 >> 1) * 1024 + (16 * mt2 + rr) * 32 + (p4 & 1) * 16;
    u32x4 u[5];
#pragma unroll
    for (int i = 0; i < 5; ++i) u[i] = u32x4{~0u, ~0u, ~0u, ~0u};
    for (int s = SWG; s < NSEG; ++s) {
      TSDECL;
      const unsigned ro = OFF_R + s * REC;
      unsigned pend = 0;
#pragma unroll
      for (int i = 0; i < 5; ++i) pend |= (PENDING(u[i]) != 0 ? 1u : 0u) << i;
      if (pend == 0x1Fu && (s + 1 < NSEG || (LASTW & 2))) wait_word(rs, ro + R_H + (16 * mt2 + 4 * w) * 32, budget);
      TS(0);
      if (pend) poll_n<5>(rs, u, [=](int) { return hvo2; }, [=](int i) { return ro + R_H + i * 8192; }, budget, pend);
      TS(1);
      ln_fast<5>(u, [](float z) { return xsum32(xsum16(xsum2(xsum1(z)))); });
      const unsigned lh = l0 + (s & 1) * XN_SEG;
#pragma unroll
      for (int i = 0; i < 5; ++i) ldsw128(lh + (4 * i + g) * 1024 + (p4 * 16 + rr) * 16, u[i]);
      wait_fill(rs, s, budget);
      if (s + 1 < NSEG) {
#pragma unroll
        for (int i = 0; i < 5; ++i) u[i] = ld16(rs, hvo2, ro + REC + R_H + i * 8192);
      }
      BAR_LDS();
      TS(2);
      f32x4 ag, au;
      bf16x8 xf[20];
#pragma unroll
      for (int ks = 0; ks < 8; ++ks) xf[ks] = asb8(ldsr128(lh + ks * 1024 + lane * 16));
#pragma unroll
      for (int ks = 0; ks < 20; ++ks) {
        if (ks + 8 < 20) xf[ks + 8] = asb8(ldsr128(lh + (ks + 8) * 1024 + lane * 16));
        __builtin_amdgcn_sched_barrier(0);
        if (ks == 0) mf2<true, true, true>(ag, au, W[ks][0], W[ks][1], xf[ks]); else mf2<true, true, false>(ag, au, W[ks][0], W[ks][1], xf[ks]);
        __builtin_amdgcn_sched_barrier(0);
      }
      NOPS2(7, ag, au);
      float a[4];
#pragma unroll
      for (int i = 0; i < 4; ++i) { const float gt = rbf(ag[i]), up = rbf(au[i]); a[i] = (gt > 0.f ? gt : gt * 0.f) * up; }
      st8(rs, mt2 * 1024 + ((2 * (w & 1) + (g >> 1)) * 16 + c16) * 16 + (g & 1) * 8, ro + R_ACT + (2 * j + (w >> 1)) * 2048,
          u32x2{nosent(cvtpk(a[0], a[1])), nosent(cvtpk(a[2], a[3]))});
      TS(3);
      TSFLUSH(s);
    }
  } else if ((b >= 235 && b < 255) || (b >= 136 && b < 156)) {
    const int mt2 = b < 235 ? 1 : 0, d = b - (mt2 ? 136 : 235);
#pragma unroll
    for (int i = 0; i < 27; ++i) glds16(Wd + (size_t)((w * 27 + i) * 16 + (lane >> 2)) * D_ + 32 * d + (lane & 3) * 8, l0 + (w * 27 + i) * 1024);
    BAR_ALL();
    const int k0 = w < 2 ? 14 * w : 13 * w + 2;
    bf16x8 W[14][2];
#pragma unroll
    for (int kk = 0; kk < 14; ++kk)
#pragma unroll
      for (int nt = 0; nt < 2; ++nt) {
        const int ks = (kk == 13 && w >= 2) ? k0 : k0 + kk;
        const unsigned a = l0 + (32 * ks + 8 * g + q4) * 64 + (16 * nt + 4 * p4) * 2;
        W[kk][nt] = cat8(trread(a), trread(a + 256));
      }
    BAR_LDS();
    for (int s = mt2 ? SWD : 0; s < SWD; ++s) {
      TSDECL;
      const unsigned ro = OFF_R + s * REC;
      wait_word(rs, ro + R_ACT + k0 * 2048, budget);
      TS(0);
      u32x4 v[28];
      poll_n<28>(rs, v, [=](int) { return lane * 16; }, [=](int i) { return ro + R_ACT + ((i >= 26 && w >= 2) ? k0 * 2 + (i - 26) : k0 * 2 + i) * 1024; }, budget);
      u32x4 hr = {0u, 0u, 0u, 0u};
      if (w < 2) hr = ld16(rs, (g & 1) * 1024 + c16 * 32 + (g >> 1) * 16, ro + R_H + 2 * d * 1024 + w * 512);
      TS(1);
      f32x4 acc[2][2];
#pragma unroll
      for (int kk = 0; kk < 13; ++kk) {
        if (kk == 0) { mf2<true, true, true>(acc[0][0], acc[0][1], W[kk][0], W[kk][1], asb8(v[2 * kk])); mf2<true, true, true>(acc[1][0], acc[1][1], W[kk][0], W[kk][1], asb8(v[2 * kk + 1])); }
        else { mf2<true, true, false>(acc[0][0], acc[0][1], W[kk][0], W[kk][1], asb8(v[2 * kk])); mf2<true, true, false>(acc[1][0], acc[1][1], W[kk][0], W[kk][1], asb8(v[2 * kk + 1])); }
      }
      if (w < 2) { mf2<true, true, false>(acc[0][0], acc[0][1], W[13][0], W[13][1], asb8(v[26])); mf2<true, true, false>(acc[1][0], acc[1][1], W[13][0], W[13][1], asb8(v[27])); }
      NOPS4(7, acc[0][0], acc[0][1], acc[1][0], acc[1][1]);
      const unsigned lred = l0 + (s & 1) * 16384;
#pragma unroll
      for (int i = 0; i < 4; ++i) ldsw128(lred + (w * 4 + i) * 1024 + lane * 16, __builtin_bit_cast(u32x4, acc[i >> 1][i & 1]));
      BAR_LDS();
      if (w < 2) {
        f32x4 z0 = __builtin_bit_cast(f32x4, ldsr128(lred + (2 * w) * 1024 + lane * 16)), z1 = __builtin_bit_cast(f32x4, ldsr128(lred + (2 * w + 1) * 1024 + lane * 16));
#pragma unroll
        for (int i = 1; i < 4; ++i) {
          z0 += __builtin_bit_cast(f32x4, ldsr128(lred + (i * 4 + 2 * w) * 1024 + lane * 16));
          z1 += __builtin_bit_cast(f32x4, ldsr128(lred + (i * 4 + 2 * w + 1) * 1024 + lane * 16));
        }
        const u32x4 o = widen4(z0, z1);
        u32x4 z;
#pragma unroll
        for (int i = 0; i < 4; ++i) z[i] = cvtpk(bflo(hr[i]) + bflo(o[i]), bfhi(hr[i]) + bfhi(o[i]));
        *(u32x4*)(out + (size_t)(s * 32 + 16 * w + c16) * D_ + 32 * d + 16 * (g & 1) + 8 * (g >> 1)) = z;
      }
      TS(2);
      TSFLUSH(s);
      fill_share(rs, s, 40 + d, t, ws);
      wait_fill(rs, s, budget);
    }
    if (mt2) wait_fill(rs, SWD - 3, budget);
    for (int s = SWD; s < NSEG; ++s) {
      TSDECL;
      const unsigned ro = OFF_R + s * REC + mt2 * 1024;
      if (s + 1 < NSEG || (LASTW & 4)) wait_word(rs, ro + R_ACT + k0 * 2048, budget);
      TS(0);
      u32x4 v[14];
      poll_n<14>(rs, v, [=](int) { return lane * 16; }, [=](int i) { return ro + R_ACT + ((i == 13 && w >= 2) ? k0 : k0 + i) * 2048; }, budget);
      u32x4 hr = {0u, 0u, 0u, 0u};
      if (w == 0) hr = ld16(rs, (g & 1) * 1024 + c16 * 32 + (g >> 1) * 16, ro + R_H + 2 * d * 1024 - mt2 * 512);
      TS(1);
      f32x4 a0, a1;
#pragma unroll
      for (int kk = 0; kk < 13; ++kk) {
        if (kk == 0) mf2<true, true, true>(a0, a1, W[kk][0], W[kk][1], asb8(v[kk])); else mf2<true, true, false>(a0, a1, W[kk][0], W[kk][1], asb8(v[kk]));
      }
      if (w < 2) mf2<true, true, false>(a0, a1, W[13][0], W[13][1], asb8(v[13]));
      NOPS2(7, a0, a1);
      const unsigned lred = l0 + (s & 1) * 16384;
      ldsw128(lred + (w * 2) * 1024 + lane * 16, __builtin_bit_cast(u32x4, a0));
      ldsw128(lred + (w * 2 + 1) * 1024 + lane * 16, __builtin_bit_cast(u32x4, a1));
      BAR_LDS();
      if (w == 0) {
        f32x4 z0 = __builtin_bit_cast(f32x4, ldsr128(lred + lane * 16)), z1 = __builtin_bit_cast(f32x4, ldsr128(lred + 1024 + lane * 16));
#pragma unroll
        for (int i = 1; i < 4; ++i) {
          z0 += __builtin_bit_cast(f32x4, ldsr128(lred + (i * 2) * 1024 + lane * 16));
          z1 += __builtin_bit_cast(f32x4, ldsr128(lred + (i * 2 + 1) * 1024 + lane * 16));
        }
        const u32x4 o = widen4(z0, z1);
        u32x4 z;
#pragma unroll
        for (int i = 0; i < 4; ++i) z[i] = cvtpk(bflo(hr[i]) + bflo(o[i]), bfhi(hr[i]) + bfhi(o[i]));
        *(u32x4*)(out + (size_t)(s * 32 + 16 * mt2 + c16) * D_ + 32 * d + 16 * (g & 1) + 8 * (g >> 1)) = z;
      }
      TS(2);
      TSFLUSH(s);
      wait_fill(rs, s, budget);
    }
  }
}

extern "C" void kernel_launch(void* const* d_in, const int* in_sizes, int n_in, void* d_out, int out_size, void* d_ws, size_t ws_size,
                              hipStream_t stream) {
  const u16* x = (const u16*)d_in[0];
  char* ws = (char*)d_ws;
  prep_kernel<<<128 + FILL_BLOCKS + 1, 256, 0, stream>>>(x, (const u16*)d_in[1], ws);
  mega_kernel<<<256, 256, 0, stream>>>(x, (const u16*)d_in[2], (const u16*)d_in[3], (const u16*)d_in[4], (const u16*)d_in[5],
                                       (const u16*)d_in[8], (const u16*)d_in[9], (const u16*)d_in[10], (u16*)d_out, ws);
}
```

```cpp
#include <hip/hip_runtime.h>
#include <hip/hip_bf16.h>
#include <stdint.h>

typedef __bf16 bf16x8 __attribute__((ext_vector_type(8)));
typedef __bf16 bf16x16 __attribute__((ext_vector_type(16)));
typedef __bf16 bf16x4 __attribute__((ext_vector_type(4)));
typedef float f32x4 __attribute__((ext_vector_type(4)));
typedef unsigned u32x4 __attribute__((ext_vector_type(4)));
typedef unsigned u32x2 __attribute__((ext_vector_type(2)));
typedef short s16x4 __attribute__((ext_vector_type(4)));
typedef __attribute__((address_space(3))) char* lds_ptr;
typedef unsigned short u16;

#define DEVI __device__ __forceinline__

constexpr int S_ = 2048, D_ = 640, HE = 4096, F_ = 1728, NSEG = 64;
constexpr int NPRE = 8;
constexpr int NFILL = 60;
constexpr unsigned XN_SEG = 40960, QK_SEG = 32 * 16384, U_SEG = 40 * 32 * 1024;
constexpr unsigned OFF_XN = 0;
constexpr unsigned OFF_QK = OFF_XN + NSEG * XN_SEG;
constexpr unsigned OFF_U = OFF_QK + NSEG * QK_SEG;
constexpr unsigned OFF_FLG = OFF_U + NSEG * U_SEG;
constexpr unsigned OFF_R = OFF_FLG + 72 * 256;
#define RM 8
#define RP 8
constexpr unsigned R_MEM = 0, R_P = RM * 10240, R_VW = R_P + RP * 20480, R_PT = R_VW + 327680, R_H = R_PT + 32 * 2560, R_ACT = R_H + 40960,
                   REC = R_ACT + 54 * 2048;
constexpr unsigned OFF_END = OFF_R + NSEG * REC;
static_assert(OFF_R % 256 == 0 && REC % 256 == 0 && OFF_U % 256 == 0, "align");
static_assert((size_t)OFF_END + (4u << 20) <= (size_t)256 * 1024 * 1024, "ws");

typedef __bf16 bf16x2 __attribute__((ext_vector_type(2)));
typedef float f32x2 __attribute__((ext_vector_type(2)));
DEVI unsigned cvtpk(float lo, float hi) { const f32x2 v = {lo, hi}; return __builtin_bit_cast(unsigned, __builtin_convertvector(v, bf16x2)); }
DEVI float bflo(unsigned u) { return __uint_as_float(u << 16); }
DEVI float bfhi(unsigned u) { return __uint_as_float(u & 0xffff0000u); }
DEVI float rbf(float x) { return bflo(cvtpk(x, x)); }
DEVI unsigned nosent(unsigned u) { return u < 0xFFFFFFFEu ? u : 0xFFFFFFFEu; }
DEVI void glds16(const void* g, unsigned lds_base) {
  unsigned sv;
  asm volatile("s_mov_b32 %0, m0\n\ts_mov_b32 m0, %2\n\ts_nop 0\n\tglobal_load_lds_dwordx4 %1, off\n\ts_mov_b32 m0, %0" : "=&s"(sv) : "v"(g), "s"(lds_base) : "memory");
}
DEVI s16x4 trread(unsigned a) { return __builtin_amdgcn_ds_read_tr16_b64_v4i16((__attribute__((address_space(3))) s16x4*)(uintptr_t)a); }
DEVI bf16x8 cat8(s16x4 a, s16x4 b) { return __builtin_bit_cast(bf16x8, __builtin_shufflevector(a, b, 0, 1, 2, 3, 4, 5, 6, 7)); }
DEVI u32x4 ldsr128(unsigned a) { return *(__attribute__((address_space(3))) u32x4*)(uintptr_t)a; }
DEVI u32x4 ldsr128o(unsigned a) { return *(volatile __attribute__((address_space(3))) u32x4*)(uintptr_t)a; }
DEVI u32x2 ldsr64(unsigned a) { return *(__attribute__((address_space(3))) u32x2*)(uintptr_t)a; }
DEVI void ldsw128(unsigned a, u32x4 v) { *(__attribute__((address_space(3))) u32x4*)(uintptr_t)a = v; }
DEVI void ldsw64(unsigned a, u32x2 v) { *(__attribute__((address_space(3))) u32x2*)(uintptr_t)a = v; }
DEVI bf16x8 asb8(u32x4 v) { return __builtin_bit_cast(bf16x8, v); }
#define MFMA16(a, b, c) __builtin_amdgcn_mfma_f32_16x16x32_bf16(a, b, c, 0, 0, 0)
#define SMF(a, b, c, idx) __builtin_amdgcn_smfmac_f32_16x16x64_bf16(a, b, c, idx, 0, 0)
#define BAR_ALL() asm volatile("s_waitcnt vmcnt(0) lgkmcnt(0)\n\ts_barrier" ::: "memory")
#define BAR_LDS() asm volatile("s_waitcnt lgkmcnt(0)\n\ts_barrier" ::: "memory")
DEVI float xmax16(float x) { auto r = __builtin_amdgcn_permlane16_swap(__float_as_uint(x), __float_as_uint(x), false, false); return fmaxf(__uint_as_float(r[0]), __uint_as_float(r[1])); }
DEVI float xmax32(float x) { auto r = __builtin_amdgcn_permlane32_swap(__float_as_uint(x), __float_as_uint(x), false, false); return fmaxf(__uint_as_float(r[0]), __uint_as_float(r[1])); }
DEVI float xsum16(float x) { auto r = __builtin_amdgcn_permlane16_swap(__float_as_uint(x), __float_as_uint(x), false, false); return __uint_as_float(r[0]) + __uint_as_float(r[1]); }
DEVI float xsum32(float x) { auto r = __builtin_amdgcn_permlane32_swap(__float_as_uint(x), __float_as_uint(x), false, false); return __uint_as_float(r[0]) + __uint_as_float(r[1]); }
DEVI float pairsum32(float a, float b) { auto r = __builtin_amdgcn_permlane32_swap(__float_as_uint(a), __float_as_uint(b), false, false); return __uint_as_float(r[0]) + __uint_as_float(r[1]); }
DEVI void st_sc1_x4(void* p, u32x4 v) { asm volatile("global_store_dwordx4 %0, %1, off sc1\n\ts_nop 1" ::"v"(p), "v"(v) : "memory"); }
DEVI void st_sc1_x2(void* p, u32x2 v) { asm volatile("global_store_dwordx2 %0, %1, off sc1\n\ts_nop 1" ::"v"(p), "v"(v) : "memory"); }
DEVI void st_sc1_x1(void* p, unsigned v) { asm volatile("global_store_dword %0, %1, off sc1\n\ts_nop 1" ::"v"(p), "v"(v) : "memory"); }
DEVI unsigned umax4(u32x4 v) { return max(max(v[0], v[1]), max(v[2], v[3])); }

typedef unsigned long long u64;
typedef __amdgpu_buffer_rsrc_t rsrc_t;
DEVI u32x4 ld16(rsrc_t rs, unsigned v, unsigned s) { return __builtin_amdgcn_raw_buffer_load_b128(rs, v, s, 16); }
DEVI unsigned ld4(rsrc_t rs, unsigned v, unsigned s) { return __builtin_amdgcn_raw_buffer_load_b32(rs, v, s, 16); }
DEVI void st16(rsrc_t rs, unsigned v, unsigned s, u32x4 d) { __builtin_amdgcn_raw_buffer_store_b128(d, rs, v, s, 16); }
DEVI void st8(rsrc_t rs, unsigned v, unsigned s, u32x2 d) { __builtin_amdgcn_raw_buffer_store_b64(d, rs, v, s, 16); }
DEVI void st4(rsrc_t rs, unsigned v, unsigned s, unsigned d) { __builtin_amdgcn_raw_buffer_store_b32(d, rs, v, s, 16); }
#define SB() __builtin_amdgcn_sched_barrier(0)
DEVI void touch(rsrc_t rs, unsigned v, unsigned s) { const unsigned x = ld4(rs, v, s); asm volatile("" :: "v"(x)); }
DEVI unsigned ror8(unsigned x) { return __builtin_amdgcn_mov_dpp(x, 0x128, 0xF, 0xF, true); }
DEVI float xmax8(float x) { return fmaxf(x, __uint_as_float(ror8(__float_as_uint(x)))); }
DEVI float xsum8(float x) { return x + __uint_as_float(ror8(__float_as_uint(x))); }
#define RED8(OP, x, y) do { float t_; asm volatile(OP " %0, %0, %2\n\ts_nop 1\n\t" OP "_dpp %0, %0, %0 row_ror:8 row_mask:0xf bank_mask:0xf\n\tv_mov_b32 %1, %0\n\ts_nop 1\n\t" \
    "v_permlane16_swap_b32 %0, %1\n\t" OP " %0, %0, %1\n\tv_mov_b32 %1, %0\n\ts_nop 1\n\tv_permlane32_swap_b32 %0, %1\n\t" OP " %0, %0, %1" : "+v"(x), "=&v"(t_) : "v"(y)); } while (0)
DEVI float vmax3(float a, float b, float c) { float r; asm("v_max3_f32 %0, %1, %2, %3" : "=v"(r) : "v"(a), "v"(b), "v"(c)); return r; }
DEVI f32x4 pairsum4(f32x4 a, f32x4 b) {
  u32x4 x, y;
#pragma unroll
  for (int i = 0; i < 4; ++i) { auto r = __builtin_amdgcn_permlane32_swap(__float_as_uint(a[i]), __float_as_uint(b[i]), false, false); x[i] = r[0]; y[i] = r[1]; }
  return __builtin_bit_cast(f32x4, x) + __builtin_bit_cast(f32x4, y);
}
#define NOMOVE() asm volatile("" ::: "memory")
#define PENDING(v) __builtin_amdgcn_ballot_w64(umax4(v) == ~0u)
DEVI float xsum2(float x) { return x + __uint_as_float(__builtin_amdgcn_update_dpp(0u, __float_as_uint(x), 0x4E, 0xF, 0xF, true)); }
DEVI float xsum1(float x) { return x + __uint_as_float(__builtin_amdgcn_update_dpp(0u, __float_as_uint(x), 0xB1, 0xF, 0xF, true)); }
DEVI u32x4 widen(unsigned a0, unsigned a1, unsigned b0, unsigned b1) {
  auto r0 = __builtin_amdgcn_permlane16_swap(a0, b0, false, false);
  auto r1 = __builtin_amdgcn_permlane16_swap(a1, b1, false, false);
  u32x4 o; o[0] = r0[0]; o[1] = r1[0]; o[2] = r0[1]; o[3] = r1[1]; return o;
}
DEVI u32x4 widen4(f32x4 x, f32x4 y) { return widen(cvtpk(x[0], x[1]), cvtpk(x[2], x[3]), cvtpk(y[0], y[1]), cvtpk(y[2], y[3])); }
DEVI u32x4 nosent4(u32x4 v) { u32x4 o; o[0] = nosent(v[0]); o[1] = nosent(v[1]); o[2] = nosent(v[2]); o[3] = nosent(v[3]); return o; }

template <int N, class RED>
DEVI void ln_apply(u32x4 (&v)[N], RED red) {
  float s = 0.f;
#pragma unroll
  for (int i = 0; i < N; ++i)
#pragma unroll
    for (int j = 0; j < 4; ++j) s += bflo(v[i][j]) + bfhi(v[i][j]);
  const float mean = red(s) * (1.0f / 640.0f);
  float q = 0.f;
#pragma unroll
  for (int i = 0; i < N; ++i)
#pragma unroll
    for (int j = 0; j < 4; ++j) { const float a = bflo(v[i][j]) - mean, b = bfhi(v[i][j]) - mean; q += a * a + b * b; }
  const float mb = rbf(mean), vb = rbf(red(q) * (1.0f / 640.0f));
  const float sd = rbf(sqrtf(rbf(vb + 1e-05f)));
#pragma unroll
  for (int i = 0; i < N; ++i)
#pragma unroll
    for (int j = 0; j < 4; ++j) v[i][j] = cvtpk(rbf(bflo(v[i][j]) - mb) / sd, rbf(bfhi(v[i][j]) - mb) / sd);
}

template <int N, class RED>
DEVI void ln_fast(u32x4 (&v)[N], RED red) {
  f32x2 s2 = {0.f, 0.f};
#pragma unroll
  for (int i = 0; i < N; ++i)
#pragma unroll
    for (int j = 0; j < 4; ++j) s2 += f32x2{bflo(v[i][j]), bfhi(v[i][j])};
  const float mean = red(s2[0] + s2[1]) * (1.0f / 640.0f);
  const float mb = rbf(mean);
  const f32x2 m2 = {mb, mb};
  f32x2 q2 = {0.f, 0.f};
#pragma unroll
  for (int i = 0; i < N; ++i)
#pragma unroll
    for (int j = 0; j < 4; ++j) { const f32x2 d = f32x2{bflo(v[i][j]), bfhi(v[i][j])} - m2; q2 += d * d; v[i][j] = cvtpk(d[0], d[1]); }
  const float dm = mean - mb;
  const float vb = rbf(fmaxf(red(q2[0] + q2[1]) * (1.0f / 640.0f) - dm * dm, 0.f));
  const float r = 1.0f / rbf(sqrtf(rbf(vb + 1e-05f)));
  const f32x2 r2 = {r, r};
#pragma unroll
  for (int i = 0; i < N; ++i)
#pragma unroll
    for (int j = 0; j < 4; ++j) { const f32x2 d = f32x2{bflo(v[i][j]), bfhi(v[i][j])} * r2; v[i][j] = cvtpk(d[0], d[1]); }
}

constexpr int FILL_BLOCKS = 256;
__global__ __launch_bounds__(256) void prep_kernel(const u16* __restrict__ x, const u16* __restrict__ mem0, char* __restrict__ ws) {
  const int b = blockIdx.x, t = threadIdx.x;
  if (b < 128) {
    const int row = b * 16 + (t >> 4), i = t & 15;
    u32x4 v[5];
#pragma unroll
    for (int j = 0; j < 5; ++j) v[j] = *(const u32x4*)(x + (size_t)row * D_ + (i + 16 * j) * 8);
    ln_apply<5>(v, [](float s) { s += __shfl_xor(s, 1); s += __shfl_xor(s, 2); s += __shfl_xor(s, 4); s += __shfl_xor(s, 8); return s; });
    char* dst = ws + OFF_XN + (size_t)(row >> 5) * XN_SEG + ((row >> 4) & 1) * 1024 + ((i & 3) * 16 + (row & 15)) * 16;
#pragma unroll
    for (int j = 0; j < 5; ++j) *(u32x4*)(dst + (4 * j + (i >> 2)) * 2048) = v[j];
  } else if (b < 128 + FILL_BLOCKS) {
    u32x4* p = (u32x4*)(ws + OFF_R + R_P);
    const unsigned n = (NPRE * REC - R_P) / 16;
    const u32x4 s = {~0u, ~0u, ~0u, ~0u};
    for (unsigned i = (b - 128) * 256 + t; i < n; i += FILL_BLOCKS * 256) p[i] = s;
    if ((b - 128) * 256 + t < QK_SEG / 16) ((u32x4*)(ws + OFF_QK))[(b - 128) * 256 + t] = s;
  } else {
    if (t < 72) ((unsigned*)(ws + OFF_FLG))[t * 64] = 0u;
    for (int i = t; i < 640; i += 256) {
      const int ct = i >> 4, bb = (i >> 3) & 1, r = i & 7;
      const u32x4 m = nosent4(*(const u32x4*)(mem0 + r * 640 + ct * 16 + bb * 8));
#pragma unroll
      for (int j = 0; j < RM; ++j) *(u32x4*)(ws + OFF_R + R_MEM + j * 10240 + (size_t)i * 16) = m;
    }
  }
}

constexpr float SCALE = 0.08838834764831845f, LOG2E = 1.4426950408889634f;
constexpr unsigned SPIN_BUDGET = 1u << 22;
constexpr int CH_STG = 65536;
constexpr int CH_MEM = 0, CH_X = CH_MEM + 10240, CH_LDS = 2 * CH_STG;

#define PSLP 1
#define PCAP 6
#define PCAPC 49
#define STR2(x) #x
#define STR(x) STR2(x)
#define CLOB3 "v244", "v245", "v246", "v247", "v248", "v249", "v250", "v251", "v252", "v253", "v254", "v255"
#define CLOB7 "v220", "v221", "v222", "v223", "v224", "v225", "v226", "v227", "v228", "v229", "v230", "v231", "v232", "v233", "v234", "v235", "v236", "v237", "v238", "v239", "v240", "v241", "v242", "v243", CLOB3
#define PEND_ASM(t, m, r0, r1, r2, r3) "v_max_u32 " t ", v" #r2 ", v" #r3 "\n\tv_max3_u32 " t ", v" #r0 ", v" #r1 ", " t "\n\tv_cmp_eq_u32 " m ", -1, " t "\n\t"
#define CHK3 "s_sleep " STR(PSLP) "\n\t" PEND_ASM("%[t]", "%[m0]", 244, 245, 246, 247) PEND_ASM("%[t]", "%[m1]", 248, 249, 250, 251) \
             PEND_ASM("%[t]", "%[m2]", 252, 253, 254, 255) "s_or_b64 %[x], %[m0], %[m1]\n\ts_or_b64 %[x], %[x], %[m2]\n\ts_cbranch_scc0 .Lpd%=\n\t"
#define CHK3N CHK3 CHK3 CHK3
DEVI void poll_mem(const char* memp, unsigned lmem, int w, int lane, unsigned& budget) {
  const char* p0 = memp + w * 2560 + lane * 16;
  const unsigned la = lmem + w * 2560 + lane * 16;
  u64 m0, m1, m2, x, sv; unsigned t, bud = __builtin_amdgcn_readfirstlane(budget);
  asm volatile("s_mov_b64 %[sv], exec\n\ts_mov_b64 %[m0], -1\n\ts_mov_b64 %[m1], -1\n\ts_mov_b64 %[m2], 0xffffffff\n"
               ".Lpl%=:\n\ts_waitcnt vmcnt(" STR(PCAP) ")\n\t"
               "s_mov_b64 exec, %[m0]\n\tglobal_load_dwordx4 v[244:247], %[p0], off sc1\n\t"
               "s_mov_b64 exec, %[m1]\n\tglobal_load_dwordx4 v[248:251], %[p0], off offset:1024 sc1\n\t"
               "s_mov_b64 exec, %[m2]\n\tglobal_load_dwordx4 v[252:255], %[p0], off offset:2048 sc1\n\t"
               "s_mov_b64 exec, %[sv]\n\t"
               CHK3N
               "s_sub_u32 %[bud], %[bud], 1\n\ts_cbranch_scc0 .Lpl%=\n\ts_mov_b32 %[bud], 0\n"
               ".Lpd%=:\n\t"
               "ds_write_b128 %[la], v[244:247]\n\tds_write_b128 %[la], v[248:251] offset:1024\n\t"
               "s_mov_b64 exec, 0xffffffff\n\tds_write_b128 %[la], v[252:255] offset:2048\n\ts_mov_b64 exec, %[sv]\n\t"
               "s_waitcnt lgkmcnt(0)\n\ts_barrier"
               : [m0] "=&s"(m0), [m1] "=&s"(m1), [m2] "=&s"(m2), [x] "=&s"(x), [sv] "=&s"(sv), [t] "=&v"(t), [bud] "+s"(bud)
               : [p0] "v"(p0), [la] "v"(la) : "memory", "scc", CLOB3);
  budget = bud;
}
#define VM0() __builtin_amdgcn_s_waitcnt(0x0F70)
DEVI void drain_mem(int lane) {
  const unsigned z = lane < 32 ? ~0u : 0u;
  VM0();
  asm volatile("s_waitcnt vmcnt(0)\n\tv_mov_b32 v244, -1\n\tv_mov_b32 v245, -1\n\tv_mov_b32 v246, -1\n\tv_mov_b32 v247, -1\n\t"
               "v_mov_b32 v248, -1\n\tv_mov_b32 v249, -1\n\tv_mov_b32 v250, -1\n\tv_mov_b32 v251, -1\n\t"
               "v_mov_b32 v252, %0\n\tv_mov_b32 v253, %0\n\tv_mov_b32 v254, %0\n\tv_mov_b32 v255, %0" :: "v"(z) : "memory", CLOB3);
}
DEVI void wait_prog(rsrc_t rs, unsigned voff, unsigned need, unsigned& seen, unsigned& budget) {
  while (__builtin_amdgcn_ballot_w64(seen < need) != 0 && budget != 0) {
    NOMOVE();
    seen = ld4(rs, voff, OFF_FLG);
    --budget;
    if (__builtin_amdgcn_ballot_w64(seen < need) != 0) __builtin_amdgcn_s_sleep(8);
  }
  NOMOVE();
}
DEVI void wait_fill(rsrc_t rs, int s, unsigned& budget) {
  if ((s & (NPRE - 1)) == NPRE - 3 && s + NPRE < NSEG) { unsigned seen = 0; wait_prog(rs, (64 + s / NPRE) * 256, NFILL, seen, budget); }
}
DEVI void wait_word(rsrc_t rs, unsigned soff, unsigned& budget) {
  for (;;) {
    NOMOVE();
    const unsigned v = ld4(rs, 0, soff);
    if (__builtin_amdgcn_readfirstlane(v) != ~0u || budget == 0) break;
    --budget;
    __builtin_amdgcn_s_sleep(2);
  }
  NOMOVE();
}
template <int N, class OFFV, class OFFS>
DEVI void poll_n(rsrc_t rs, u32x4 (&v)[N], OFFV offv, OFFS offs, unsigned& budget, unsigned pend = (1u << N) - 1u) {
  for (;;) {
    NOMOVE();
#pragma unroll
    for (int i = 0; i < N; ++i) if (pend & (1u << i)) v[i] = ld16(rs, offv(i), offs(i));
    unsigned np = 0;
#pragma unroll
    for (int i = 0; i < N; ++i) np |= (PENDING(v[i]) != 0 ? 1u : 0u) << i;
    pend = np;
    if (pend == 0 || budget == 0) break;
    --budget;
  }
  NOMOVE();
}
template <bool AG>
DEVI void smf2(f32x4& c0, f32x4& c1, bf16x8 a, const bf16x16& w0, const bf16x16& w1, int idx) {
  if (AG) asm volatile("v_smfmac_f32_16x16x64_bf16 %0, %2, %3, %5\n\tv_smfmac_f32_16x16x64_bf16 %1, %2, %4, %5" : "+v"(c0), "+v"(c1) : "v"(a), "a"(w0), "a"(w1), "v"(idx));
  else asm volatile("v_smfmac_f32_16x16x64_bf16 %0, %2, %3, %5\n\tv_smfmac_f32_16x16x64_bf16 %1, %2, %4, %5" : "+v"(c0), "+v"(c1) : "v"(a), "v"(w0), "v"(w1), "v"(idx));
}
template <bool AG, bool WA, bool FIRST>
DEVI void mf2(f32x4& c0, f32x4& c1, const bf16x8& w0, const bf16x8& w1, bf16x8 x) {
#define MF2_ASM(T, CO, WC) asm volatile(T : CO(c0), CO(c1) : WC(w0), WC(w1), "v"(x))
#define T_WA_F "v_mfma_f32_16x16x32_bf16 %0, %2, %4, 0\n\tv_mfma_f32_16x16x32_bf16 %1, %3, %4, 0"
#define T_WA_N "v_mfma_f32_16x16x32_bf16 %0, %2, %4, %0\n\tv_mfma_f32_16x16x32_bf16 %1, %3, %4, %1"
#define T_WB_F "v_mfma_f32_16x16x32_bf16 %0, %4, %2, 0\n\tv_mfma_f32_16x16x32_bf16 %1, %4, %3, 0"
#define T_WB_N "v_mfma_f32_16x16x32_bf16 %0, %4, %2, %0\n\tv_mfma_f32_16x16x32_bf16 %1, %4, %3, %1"
  if (AG) {
    if (WA) { if (FIRST) MF2_ASM(T_WA_F, "=&v", "a"); else MF2_ASM(T_WA_N, "+v", "a"); }
    else { if (FIRST) MF2_ASM(T_WB_F, "=&v", "a"); else MF2_ASM(T_WB_N, "+v", "a"); }
  } else {
    if (WA) { if (FIRST) MF2_ASM(T_WA_F, "=&v", "v"); else MF2_ASM(T_WA_N, "+v", "v"); }
    else { if (FIRST) MF2_ASM(T_WB_F, "=&v", "v"); else MF2_ASM(T_WB_N, "+v", "v"); }
  }
}
template <bool FIRST>
DEVI void mf1(f32x4& c, u32x4 a, bf16x8 b) {
  if (FIRST) asm volatile("v_mfma_f32_16x16x32_bf16 %0, %1, %2, 0" : "=&v"(c) : "v"(a), "v"(b));
  else asm volatile("v_mfma_f32_16x16x32_bf16 %0, %1, %2, %0" : "+v"(c) : "v"(a), "v"(b));
}
#define DLY_CU 60
#define SWG 40
#define SWD 48
#define DA 170
#define DB 195
#define DC 200
#define PNOM 312
struct Pace {
  unsigned last, per, until;
  DEVI void mark(int d) {
    const unsigned now = (unsigned)__builtin_amdgcn_s_memrealtime(), p = now - last;
    until = now + (unsigned)max(d + min((int)min(min(p, per), 1000u) - PNOM, 0), 0);
    last = now; per = p;
  }
  DEVI void pause() const { while ((int)((unsigned)__builtin_amdgcn_s_memrealtime() - until) < 0) __builtin_amdgcn_s_sleep(1); }
};
DEVI void delay_ticks(unsigned n) { const u64 t = __builtin_amdgcn_s_memrealtime() + n; while (__builtin_amdgcn_s_memrealtime() < t) __builtin_amdgcn_s_sleep(4); }
#define NOPS2(n, c0, c1) asm volatile("s_nop " #n : "+v"(c0), "+v"(c1))
#define NOPS4(n, c0, c1, c2, c3) asm volatile("s_nop " #n : "+v"(c0), "+v"(c1), "+v"(c2), "+v"(c3))
constexpr int NVS = 5;

template <bool SP> DEVI int swz(int row) { return SP ? (((row & 1) << 1) | (row & 12)) : (((row & 3) << 2) | ((row >> 2) & 3)); }
template <int NSL, bool SP, int NMID, class SRC, class PUT, class MID>
DEVI void load_slabs(unsigned l0, int w, int lane, int rbase, int rstep, int col0, int colt, SRC src, PUT put, MID mid) {
  constexpr int NB = (NSL + 3) / 4;
  auto stage = [&](int bt) {
#pragma unroll
    for (int j = 0; j < 16; ++j) {
      const int sl = bt * 4 + (j >> 2), blk = w * 4 + (j & 3), pos = blk * 64 + lane;
      if (sl < NSL) glds16(src(sl, pos >> 4, (pos & 15) ^ swz<SP>(pos >> 4)), l0 + (bt & 1) * CH_STG + (j >> 2) * 16384 + blk * 1024);
    }
  };
  stage(0);
#pragma unroll
  for (int bt = 0; bt < NB; ++bt) {
    if (bt + 1 < NB) {
      stage(bt + 1);
      if ((bt + 2) * 4 <= NSL) asm volatile("s_waitcnt vmcnt(16)\n\ts_barrier" ::: "memory");
      else asm volatile("s_waitcnt vmcnt(8)\n\ts_barrier" ::: "memory");
    } else asm volatile("s_waitcnt vmcnt(%0)\n\ts_barrier" :: "n"(NMID) : "memory");
#pragma unroll
    for (int j = 0; j < 4; ++j)
#pragma unroll
      for (int tt = 0; tt < 2; ++tt) {
        if (bt * 4 + j < NSL) {
          s16x4 r[4];
#pragma unroll
          for (int i = 0; i < 4; ++i) {
            const int row = 32 * (i >> 1) + rbase + rstep * (i & 1), cb = (col0 + colt * tt) * 2;
            r[i] = trread(l0 + (bt & 1) * CH_STG + j * 16384 + row * 256 + ((((cb >> 4) ^ swz<SP>(row)) << 4) | (cb & 15)));
          }
          put(bt * 4 + j, tt, cat8(r[0], r[1]), cat8(r[2], r[3]));
        }
      }
    BAR_LDS();
    if (bt + 2 == NB) mid();
  }
}
DEVI const u16* head_src(const u16* p0, const u16* p1, bool qk, int h, int sl, int row, int ch) {
  if (sl < 10) return p0 + (size_t)(sl * 64 + row) * HE + h * 128 + ch * 8;
  const int o = qk ? (sl - 10) * 64 * HE + h * 128 : (h * 128 + ((sl - 10) & 1) * 64) * D_ + ((sl - 10) >> 1) * 128;
  return p1 + o + row * (qk ? HE : D_) + ch * 8;
}
DEVI void fill_share(rsrc_t rs, int s, int idx, int t, char* ws) {
  if ((s & 3) != 0 || (s / 4 + 1) * NPRE >= NSEG) return;
  const int c = s / 4;
  const u32x4 sv = {~0u, ~0u, ~0u, ~0u};
  constexpr unsigned n = NPRE * REC / 4096;
  static_assert(NPRE * REC % 4096 == 0, "fill");
  for (unsigned i = idx; i < n; i += NFILL) st16(rs, t * 16, OFF_R + (c + 1) * NPRE * REC + i * 4096, sv);
  BAR_ALL();
  if (t == 0) __hip_atomic_fetch_add((unsigned*)(ws + OFF_FLG) + (64 + c) * 64, 1u, __ATOMIC_RELAXED, __HIP_MEMORY_SCOPE_AGENT);
}

__global__ __launch_bounds__(256, 1) void mega_kernel(const u16* __restrict__ x, const u16* __restrict__ Wq, const u16* __restrict__ Wk,
                                                       const u16* __restrict__ Wv, const u16* __restrict__ Wo, const u16* __restrict__ Wg,
                                                       const u16* __restrict__ Wu, const u16* __restrict__ Wd, u16* __restrict__ out, char* ws) {
  __shared__ __attribute__((aligned(16))) char smem[CH_LDS];
  const unsigned l0 = (unsigned)(uintptr_t)(lds_ptr)smem;
  const int t = threadIdx.x, lane = t & 63, w = __builtin_amdgcn_readfirstlane(t >> 6);
  const int b = blockIdx.x;
  unsigned budget = SPIN_BUDGET;
  Pace pace = {0u, 0u, 0u};
  const rsrc_t rs = __builtin_amdgcn_make_buffer_rsrc((void*)ws, 0, 0x10000000, 0x00020000);
  char* const rec0 = ws + OFF_R;
  const int g = lane >> 4, c16 = lane & 15, q4 = c16 >> 2, p4 = lane & 3;
  const int gp = 2 * (g & 1) + (g >> 1);
  if (b < 64) {
    const bool roleA = b < 32;
    const int h = b & 31;
    bf16x16 W[20][2];
    const u16* const p0 = roleA ? Wq : Wv; const u16* const p1 = roleA ? Wk : Wo;
    load_slabs<20, true, 0>(l0, w, lane, 16 * (g >> 1) + 4 * (g & 1) + (q4 & 1) + 8 * (q4 >> 1), 2, 32 * w + 4 * p4, 16,
                   [=](int sl, int row, int ch) { return head_src(p0, p1, roleA, h, sl, row, ch); },
                   [&](int sl, int tt, bf16x8 lo, bf16x8 hi) { W[sl][tt] = __builtin_shufflevector(lo, hi, 0, 1, 2, 3, 4, 5, 6, 7, 8, 9, 10, 11, 12, 13, 14, 15); }, [] {});
    const int sidx = (lane & 8) ? 0xEEEE : 0x4444;
    const unsigned lmem = l0 + CH_MEM, lx = l0 + CH_X;
    drain_mem(lane);

    if (roleA) {
      u32x4 kq[12] = {};
      unsigned seen = 0;
      const unsigned qoff = w == 2 ? 4096 : 0;
      auto fetch_qk = [&](int s) {
        wait_prog(rs, h * 256, s + 1, seen, budget);
        const unsigned so = OFF_QK + (s * 32 + h) * 16384;
#pragma unroll
        for (int i = 0; i < 12; ++i) kq[i] = ld16(rs, lane * 16, so + (i < 8 ? 8192 + i * 1024 : qoff + (i - 8) * 1024));
      };
      const int lim = 16 * (w - 1) + c16;
      const unsigned xw = (32 * w + 16 * (lane >> 5) + c16) * 16 + (g & 1) * 8, xr = (8 * g + q4) * 16 + (p4 & 1) * 8;
      VM0();
      for (int s = 0; s < NSEG; ++s) {
        char* const rec = rec0 + (size_t)s * REC;
        f32x4 aq0 = {0.f, 0.f, 0.f, 0.f}, aq1 = aq0, ak0 = aq0, ak1 = aq0;
        NOPS2(0, aq0, aq1); NOPS2(0, ak0, ak1);
        poll_mem(rec + R_MEM + (b % RM) * 10240, lmem, w, lane, budget);
        bf16x8 af[10];
#pragma unroll
        for (int ks = 0; ks < 10; ++ks) af[ks] = asb8(ldsr128o(lmem + ks * 1024 + lane * 16));
        SB();
#pragma unroll
        for (int ks = 0; ks < 10; ++ks) {
          if (ks < NVS) smf2<false>(aq0, aq1, af[ks], W[ks][0], W[ks][1], sidx); else smf2<true>(aq0, aq1, af[ks], W[ks][0], W[ks][1], sidx);
        }
        smf2<true>(ak0, ak1, af[0], W[10][0], W[10][1], sidx);
        smf2<true>(ak0, ak1, af[1], W[11][0], W[11][1], sidx);
        NOPS2(3, aq0, aq1);
        {
          const f32x4 z = pairsum4(aq0, aq1);
          ldsw64(lx + xw, u32x2{cvtpk(z[0], z[1]), cvtpk(z[2], z[3])});
        }
        BAR_LDS();
        if (__builtin_expect(s == 0 && w < 3, 0)) {
          poll_n<12>(rs, kq, [=](int) { return lane * 16; }, [=](int i) { return OFF_QK + h * 16384 + (i < 8 ? 8192 + i * 1024 : qoff + (i - 8) * 1024); }, budget);
        }
        bf16x8 qb[4];
        if (w == 0) {
#pragma unroll
          for (int ks = 0; ks < 4; ++ks) kq[8 + ks] = __builtin_bit_cast(u32x4, cat8(trread(lx + xr + ks * 512), trread(lx + xr + ks * 512 + 64)));
        }
#pragma unroll
        for (int ks = 0; ks < 4; ++ks) qb[ks] = asb8(kq[8 + ks]);
        SB();
#pragma unroll
        for (int ks = 2; ks < 10; ++ks) smf2<true>(ak0, ak1, af[ks], W[10 + ks][0], W[10 + ks][1], sidx);
        const f32x4 zero = {0.f, 0.f, 0.f, 0.f};
        f32x4 s1, s2;
        mf1<true>(s1, kq[0], qb[0]); mf1<true>(s2, kq[4], qb[0]);
        NOPS2(5, ak0, ak1);
        {
          const f32x4 z = pairsum4(ak0, ak1);
          ldsw64(lx + 2048 + xw, u32x2{cvtpk(z[0], z[1]), cvtpk(z[2], z[3])});
        }
        SB();
        mf1<false>(s1, kq[1], qb[1]); mf1<false>(s2, kq[5], qb[1]); mf1<false>(s1, kq[2], qb[2]);
        BAR_LDS();
        bf16x8 xk[4];
#pragma unroll
        for (int ks = 0; ks < 4; ++ks) xk[ks] = cat8(trread(lx + 2048 + xr + ks * 512), trread(lx + 2048 + xr + ks * 512 + 64));
        SB();
        mf1<false>(s2, kq[6], qb[2]); mf1<false>(s1, kq[3], qb[3]); mf1<false>(s2, kq[7], qb[3]);
        NOPS2(7, s1, s2);
        if (w == 0) {
          const bool hi8 = (lane & 8) != 0;
          f32x4 s0;
          const unsigned a = cvtpk(hi8 ? s2[0] : s1[0], hi8 ? s2[1] : s1[1]), c = cvtpk(hi8 ? s2[2] : s1[2], hi8 ? s2[3] : s1[3]);
          SB();
          mf1<true>(s0, __builtin_bit_cast(u32x4, xk[0]), qb[0]);
          const f32x2 sc2 = {SCALE, SCALE}, l2 = {LOG2E, LOG2E};
          const f32x2 a1 = f32x2{bflo(a), bfhi(a)} * sc2, c1 = f32x2{bflo(c), bfhi(c)} * sc2;
          SB();
          mf1<false>(s0, __builtin_bit_cast(u32x4, xk[1]), qb[1]);
          const unsigned a2 = cvtpk(a1[0], a1[1]), c2 = cvtpk(c1[0], c1[1]);
          const f32x2 t01 = {bflo(a2), bfhi(a2)}, t23 = {bflo(c2), bfhi(c2)};
          SB();
          mf1<false>(s0, __builtin_bit_cast(u32x4, xk[2]), qb[2]);
          float mx = vmax3(t01[0], t01[1], t23[0]);
          mx = vmax3(mx, mx, t23[1]);
          SB();
          mf1<false>(s0, __builtin_bit_cast(u32x4, xk[3]), qb[3]);
          f32x2 u01 = t01 * l2, u23 = t23 * l2;
          asm volatile("s_nop 5" : "+v"(s0), "+v"(u01), "+v"(u23));
          const float vm = rbf(rbf(lane >= 32 ? (hi8 ? s0[3] : s0[2]) : (hi8 ? s0[1] : s0[0])) * SCALE);
          RED8("v_max_f32", mx, vm);
          const float ml = mx * LOG2E;
          const f32x2 ml2 = {ml, ml};
          u01 -= ml2; u23 -= ml2;
          const float em = __builtin_amdgcn_exp2f(__builtin_fmaf(vm, LOG2E, -ml));
          const f32x2 e01 = {__builtin_amdgcn_exp2f(u01[0]), __builtin_amdgcn_exp2f(u01[1])}, e23 = {__builtin_amdgcn_exp2f(u23[0]), __builtin_amdgcn_exp2f(u23[1])};
          const f32x2 es = e01 + e23;
          float sum = es[0] + em;
          RED8("v_add_f32", sum, es[1]);
          const float inv = __builtin_amdgcn_rcpf(sum);
          const f32x2 inv2 = {inv, inv}, p01 = e01 * inv2, p23 = e23 * inv2;
          const float pmv = em * inv;
          u32x4 pt;
          pt[0] = nosent(cvtpk(p01[0], p01[1])); pt[1] = nosent(cvtpk(p23[0], p23[1]));
          pt[2] = ror8(pt[0]); pt[3] = ror8(pt[1]);
          const unsigned d0 = nosent(cvtpk(pmv, __uint_as_float(ror8(__float_as_uint(pmv)))));
          const unsigned dm = __builtin_amdgcn_update_dpp(d0, d0, 0x128, 0xF, 0xC, false);
          auto rm = __builtin_amdgcn_permlane32_swap(dm, dm, false, false);
          const unsigned po = OFF_R + s * REC + R_P + h * 640;
#pragma unroll
          for (int j = 0; j < RP / 2; ++j) st16(rs, (c16 >> 3) * 20480 + (c16 & 7) * 64 + g * 16, po + j * 40960, pt);
#pragma unroll
          for (int j = 0; j < RP / 4; ++j) st8(rs, ((c16 >> 3) + (g >> 1) * 2) * 20480 + 512 + (c16 & 7) * 16 + (g & 1) * 8, po + j * 81920, u32x2{rm[0], rm[1]});
          pace.mark(DA);
        } else {
          if (w < 3) {
            f32x4 s0 = MFMA16(xk[0], qb[0], zero);
            s0 = MFMA16(xk[1], qb[1], s0); s0 = MFMA16(xk[2], qb[2], s0); s0 = MFMA16(xk[3], qb[3], s0);
            float v[12];
#pragma unroll
            for (int i = 0; i < 4; ++i) {
              v[i] = g < 2 ? rbf(rbf(s0[i]) * SCALE) : -INFINITY;
              v[4 + i] = (4 * g + i <= lim) ? rbf(rbf(s1[i]) * SCALE) : -INFINITY;
              v[8 + i] = (16 + 4 * g + i <= lim) ? rbf(rbf(s2[i]) * SCALE) : -INFINITY;
            }
            float mx = v[0];
#pragma unroll
            for (int i = 1; i < 12; ++i) mx = fmaxf(mx, v[i]);
            mx = xmax32(xmax16(mx));
            float sum = 0.f;
#pragma unroll
            for (int i = 0; i < 12; ++i) { v[i] = __builtin_amdgcn_exp2f((v[i] - mx) * LOG2E); sum += v[i]; }
            sum = xsum32(xsum16(sum));
            const float inv = __builtin_amdgcn_rcpf(sum);
            u32x4 pt; u32x2 pm;
            pm[0] = nosent(cvtpk(v[0] * inv, v[1] * inv)); pm[1] = nosent(cvtpk(v[2] * inv, v[3] * inv));
            pt[0] = nosent(cvtpk(v[4] * inv, v[5] * inv)); pt[1] = nosent(cvtpk(v[6] * inv, v[7] * inv));
            pt[2] = nosent(cvtpk(v[8] * inv, v[9] * inv)); pt[3] = nosent(cvtpk(v[10] * inv, v[11] * inv));
            char* pd = rec + R_PT + h * 2560;
            st_sc1_x4(pd + (w - 1) * 1024 + lane * 16, pt);
            if (g < 2) st_sc1_x2(pd + 2048 + (16 * (w - 1) + c16) * 16 + g * 8, pm);
            pace.mark(DA - 15);
          } else pace.mark(DA + 25);
        }
        if (w < 3 && s + 1 < NSEG) fetch_qk(s + 1);
        if (s + 1 < NSEG) touch(rs, w == 0 ? R_P + (lane & 7) * 20480 + h * 640 : R_PT + h * 2560, OFF_R + (s + 1) * REC);
        drain_mem(lane);
        wait_fill(rs, s, budget);
        pace.pause();
      }
    } else {
      for (int s = 0; s < NSEG; ++s) {
        char* const rec = rec0 + (size_t)s * REC;
        f32x4 a0 = {0.f, 0.f, 0.f, 0.f}, a1 = a0;
        f32x4 c[5][2];
#pragma unroll
        for (int cb = 0; cb < 5; ++cb) { c[cb][0] = a0; c[cb][1] = a0; NOPS2(0, c[cb][0], c[cb][1]); }
        NOPS2(0, a0, a1);
        poll_mem(rec + R_MEM + (b % RM) * 10240, lmem, w, lane, budget);
        bf16x8 af[10];
#pragma unroll
        for (int ks = 0; ks < 10; ++ks) af[ks] = asb8(ldsr128o(lmem + ks * 1024 + lane * 16));
        __builtin_amdgcn_sched_barrier(0);
#pragma unroll
        for (int ks = 0; ks < 10; ++ks) {
          if (ks < NVS) smf2<false>(a0, a1, af[ks], W[ks][0], W[ks][1], sidx); else smf2<true>(a0, a1, af[ks], W[ks][0], W[ks][1], sidx);
        }
        NOPS2(7, a0, a1);
        {
          const f32x4 z = pairsum4(a0, a1);
          const u32x2 o = {cvtpk(z[0], z[1]), cvtpk(z[2], z[3])};
          ldsw64(lx + (32 * w + 16 * (lane >> 5) + c16) * 16 + (g & 1) * 8, o);
        }
        BAR_LDS();
        bf16x8 va[2];
#pragma unroll
        for (int ks = 0; ks < 2; ++ks) {
          const s16x4 x0 = trread(lx + (64 * ks + 16 * g + 8 * (p4 >> 1) + q4) * 16 + (p4 & 1) * 8);
          const s16x4 x1 = trread(lx + (64 * ks + 16 * g + 8 * (p4 >> 1) + 4 + q4) * 16 + (p4 & 1) * 8);
          va[ks] = cat8(x0, x1);
        }
        char* vd = rec + R_VW + (size_t)(2 * w + (lane >> 5)) * 8192 + h * 256 + c16 * 16 + (g & 1) * 8;
#pragma unroll
        for (int cb = 0; cb < 6; ++cb) {
          if (cb < 5) {
            smf2<true>(c[cb][0], c[cb][1], va[0], W[10 + 2 * cb][0], W[10 + 2 * cb][1], sidx);
            smf2<true>(c[cb][0], c[cb][1], va[1], W[11 + 2 * cb][0], W[11 + 2 * cb][1], sidx);
          }
          if (cb > 0) {
            f32x4 c0 = c[cb - 1][0], c1 = c[cb - 1][1];
            if (cb < 5) NOPS2(3, c0, c1); else NOPS2(7, c0, c1);
            const f32x4 z = pairsum4(c0, c1);
            const u32x2 o = {nosent(cvtpk(z[0], z[1])), nosent(cvtpk(z[2], z[3]))};
            st_sc1_x2(vd + (cb - 1) * 65536, o);
          }
        }
        pace.mark(DB);
        if (s + 1 < NSEG) touch(rs, R_VW + (2 * w + (lane >> 5)) * 8192 + h * 256 + min(lane & 7, 4) * 65536, OFF_R + (s + 1) * REC);
        drain_mem(lane);
        wait_fill(rs, s, budget);
        pace.pause();
      }
    }
  } else if (b < 104) {
    const int ct = b - 64;
    u32x4 uf[8];
    unsigned seen = 0;
    auto fetch_u = [&](int s) {
      wait_prog(rs, (32 + 8 * w + (lane & 7)) * 256, s + 1, seen, budget);
#pragma unroll
      for (int i = 0; i < 8; ++i) uf[i] = ld16(rs, lane * 16, OFF_U + ((s * 40 + ct) * 32 + 8 * w + i) * 1024);
    };
    const int r = lane & 7, hb = (lane >> 3) & 1;
#define PSLPC 1
#define CHK7 "s_sleep " STR(PSLPC) "\n\t" PEND_ASM("%[t]", "%[k5]", 248, 249, 250, 251) PEND_ASM("%[t]", "%[k6]", 252, 253, 254, 255) \
             PEND_ASM("%[t]", "%[k0]", 228, 229, 230, 231) "s_or_b64 %[x], %[k5], %[k6]\n\t" PEND_ASM("%[t]", "%[k1]", 232, 233, 234, 235) "s_or_b64 %[x], %[x], %[k0]\n\t" \
             PEND_ASM("%[t]", "%[k2]", 236, 237, 238, 239) "s_or_b64 %[x], %[x], %[k1]\n\t" PEND_ASM("%[t]", "%[k3]", 240, 241, 242, 243) "s_or_b64 %[x], %[x], %[k2]\n\t" \
             PEND_ASM("%[t]", "%[k4]", 244, 245, 246, 247) "s_or_b64 %[x], %[x], %[k3]\n\ts_or_b64 %[x], %[x], %[k4]\n\ts_cbranch_scc0 .Lcd%=\n\t"
#define CHK7N CHK7 CHK7 CHK7 CHK7
#define CHK7F CHK7 CHK7
#define ROUND7 "s_mov_b64 exec, %[k5]\n\tglobal_load_dwordx4 v[248:251], %[vp], off sc1\n\t" \
               "s_mov_b64 exec, %[k6]\n\tglobal_load_dwordx4 v[252:255], %[vp], off offset:1024 sc1\n\t" \
               "s_mov_b64 exec, %[k0]\n\tglobal_load_dwordx4 v[228:231], %[pt], off sc1\n\t" \
               "s_mov_b64 exec, %[k1]\n\tglobal_load_dwordx4 v[232:235], %[pt], off offset:1280 sc1\n\t" \
               "s_mov_b64 exec, %[k2]\n\tglobal_load_dwordx4 v[236:239], %[pt], off offset:2560 sc1\n\t" \
               "s_mov_b64 exec, %[k3]\n\tglobal_load_dwordx4 v[240:243], %[pt], off offset:3840 sc1\n\t" \
               "s_mov_b64 exec, %[k4]\n\tglobal_load_dwordx4 v[244:247], %[pm], off sc1\n\t" \
               "s_mov_b64 exec, %[sv]\n\t"
#define M4(a, b, c, d) "v_mov_b32 v" #a ", -1\n\tv_mov_b32 v" #b ", -1\n\tv_mov_b32 v" #c ", -1\n\tv_mov_b32 v" #d ", -1\n\t"
#define DRAIN7() do { VM0(); asm volatile("s_waitcnt vmcnt(0)\n\t" M4(228, 229, 230, 231) M4(232, 233, 234, 235) M4(236, 237, 238, 239) M4(240, 241, 242, 243) \
                            M4(244, 245, 246, 247) M4(248, 249, 250, 251) M4(252, 253, 254, 255) ::: "memory", CLOB7); } while (0)
    const unsigned rpo = R_P + (ct % RP) * 20480;
    auto swap_u = [&]() {
      if (ct & 1) {
#pragma unroll
        for (int i = 0; i < 8; ++i) uf[i] = u32x4{uf[i][2], uf[i][3], uf[i][0], uf[i][1]};
      }
    };
    fetch_u(0);
    DRAIN7();
    swap_u();
    for (int s = 0; s < NSEG - 1; ++s) {
      char* const rec = rec0 + (size_t)s * REC;
      const unsigned lred = l0 + (s & 1) * 4096;
      const char* pt = rec + rpo + (8 * w + hb) * 640 + r * 64 + g * 16;
      const char* pm = rec + rpo + (8 * w + 4 * hb + g) * 640 + 512 + r * 16;
      const char* vp = rec + R_VW + (size_t)ct * 8192 + 8 * w * 256 + lane * 16;
      u64 k0, k1, k2, k3, k4, k5, k6, x, sv; unsigned tmp, bud = __builtin_amdgcn_readfirstlane(budget);
      unsigned ra = lred + (g * 8 + r) * 16;
      asm volatile("" : "+v"(ra));
      const unsigned wa = ra + hb * 2048 + w * 512;
      asm volatile("s_mov_b64 %[sv], exec\n\ts_mov_b64 %[k0], -1\n\ts_mov_b64 %[k1], -1\n\ts_mov_b64 %[k2], -1\n\ts_mov_b64 %[k3], -1\n\t"
                   "s_mov_b64 %[k4], -1\n\ts_mov_b64 %[k5], -1\n\ts_mov_b64 %[k6], -1\n"
                   ROUND7 CHK7N ROUND7 CHK7N
                   ".Lcl%=:\n\ts_waitcnt vmcnt(" STR(PCAPC) ")\n\t"
                   ROUND7 CHK7F
                   "s_sub_u32 %[bud], %[bud], 1\n\ts_cbranch_scc0 .Lcl%=\n\ts_mov_b32 %[bud], 0\n"
                   ".Lcd%=:\n\t"
                   "v_mfma_f32_16x16x32_bf16 v[220:223], %[u0], v[228:231], 0\n\tv_mfma_f32_16x16x32_bf16 v[224:227], %[u1], v[228:231], 0\n\t"
                   "v_mfma_f32_16x16x32_bf16 v[220:223], %[u2], v[232:235], v[220:223]\n\tv_mfma_f32_16x16x32_bf16 v[224:227], %[u3], v[232:235], v[224:227]\n\t"
                   "v_mfma_f32_16x16x32_bf16 v[220:223], %[u4], v[236:239], v[220:223]\n\tv_mfma_f32_16x16x32_bf16 v[224:227], %[u5], v[236:239], v[224:227]\n\t"
                   "v_mfma_f32_16x16x32_bf16 v[220:223], %[u6], v[240:243], v[220:223]\n\tv_mfma_f32_16x16x32_bf16 v[224:227], %[u7], v[240:243], v[224:227]\n\t"
                   "v_mfma_f32_16x16x32_bf16 v[220:223], v[248:251], v[244:247], v[220:223]\n\tv_mfma_f32_16x16x32_bf16 v[224:227], v[252:255], v[244:247], v[224:227]\n\t"
                   "s_nop 7\n\t"
                   "v_add_f32_dpp v220, v224, v220 row_ror:8 row_mask:0xf bank_mask:0xf\n\tv_add_f32_dpp v221, v225, v221 row_ror:8 row_mask:0xf bank_mask:0xf\n\t"
                   "v_add_f32_dpp v222, v226, v222 row_ror:8 row_mask:0xf bank_mask:0xf\n\tv_add_f32_dpp v223, v227, v223 row_ror:8 row_mask:0xf bank_mask:0xf\n\t"
                   "ds_write_b128 %[wa], v[220:223]\n\ts_waitcnt lgkmcnt(0)\n\ts_barrier"
                   : [k0] "=&s"(k0), [k1] "=&s"(k1), [k2] "=&s"(k2), [k3] "=&s"(k3), [k4] "=&s"(k4), [k5] "=&s"(k5), [k6] "=&s"(k6), [x] "=&s"(x),
                     [sv] "=&s"(sv), [t] "=&v"(tmp), [bud] "+s"(bud)
                   : [wa] "v"(wa), [pt] "v"(pt), [pm] "v"(pm), [vp] "v"(vp), [u0] "v"(uf[0]), [u1] "v"(uf[1]), [u2] "v"(uf[2]), [u3] "v"(uf[3]),
                     [u4] "v"(uf[4]), [u5] "v"(uf[5]), [u6] "v"(uf[6]), [u7] "v"(uf[7]) : "memory", "scc", CLOB7);
      budget = bud;
      {
        f32x4 z = __builtin_bit_cast(f32x4, ldsr128(ra)) + __builtin_bit_cast(f32x4, ldsr128(ra + 512));
        z += __builtin_bit_cast(f32x4, ldsr128(ra + 1024)) + __builtin_bit_cast(f32x4, ldsr128(ra + 1536));
        const u32x2 o = {nosent(cvtpk(z[0], z[1])), nosent(cvtpk(z[2], z[3]))};
#pragma unroll
        for (int j = 0; j < RM / 8; ++j)
          st8(rs, hb * 10240 + (g >> 1) * 128 + r * 16 + (g & 1) * 8, OFF_R + (s + 1) * REC + R_MEM + (w * (RM / 4) + 2 * j) * 10240 + ct * 256, o);
      }
      pace.mark(DC);
      if (s > 1) delay_ticks(DLY_CU);
      if (s + 2 < NSEG) fetch_u(s + 1);
      if (s + 2 < NSEG) touch(rs, R_MEM + (2 * w + hb) * 10240 + ct * 256, OFF_R + (s + 2) * REC);
      DRAIN7();
      swap_u();
      wait_fill(rs, s, budget);
      pace.pause();
    }
  } else if (b < 168) {
    const bool qk = b < 136;
    const int h = (b - 104) & 31;
    bf16x8 W[20][2][2];
    const u16* const p0 = qk ? Wq : Wv; const u16* const p1 = qk ? Wk : Wo;
    auto xbuf = [=](int s) { return l0 + ((s & 1) ? 0u : 2 * XN_SEG); };
    auto stageX = [&](int s) {
#pragma unroll
      for (int j = 0; j < 10; ++j) glds16(ws + OFF_XN + (size_t)s * XN_SEG + (w * 10 + j) * 1024 + lane * 16, xbuf(s) + (w * 10 + j) * 1024);
    };
    load_slabs<20, false, 10>(l0, w, lane, 8 * g + q4, 4, 32 * w + 4 * p4, 16,
                   [=](int sl, int row, int ch) { return head_src(p0, p1, qk, h, sl, row, ch); },
                   [&](int sl, int tt, bf16x8 lo, bf16x8 hi) { W[sl][tt][0] = lo; W[sl][tt][1] = hi; }, [&] { stageX(0); });
    const unsigned lv = l0 + XN_SEG;
    const unsigned pword = OFF_FLG + (b - 104) * 256;
    for (int s = 0; s < NSEG; ++s) {
      if (s <= 4) asm volatile("s_waitcnt vmcnt(0)" ::: "memory");
      BAR_LDS();
      if (t == 0 && s >= 1) st4(rs, 0, pword, s <= 4 ? s : s - 1);
      if (s + 1 < NSEG) stageX(s + 1);
      const unsigned lxb = xbuf(s) + lane * 16;
      bf16x8 xa = asb8(ldsr128(lxb)), xb = asb8(ldsr128(lxb + 1024));
      if (qk) {
        f32x4 acc[2][4];
#pragma unroll
        for (int ks = 0; ks < 20; ++ks) {
          bf16x8 na = xa, nb = xb;
          if (ks + 1 < 20) { na = asb8(ldsr128(lxb + (ks + 1) * 2048)); nb = asb8(ldsr128(lxb + (ks + 1) * 2048 + 1024)); }
          __builtin_amdgcn_sched_barrier(0);
          const int sl = ks >> 1, kk = ks & 1;
#define QKSTEP(F) \
          if (sl < NVS) { mf2<false, true, F>(acc[0][0], acc[0][1], W[sl][0][kk], W[sl][1][kk], xa); mf2<false, true, F>(acc[1][0], acc[1][1], W[sl][0][kk], W[sl][1][kk], xb); } \
          else { mf2<true, true, F>(acc[0][0], acc[0][1], W[sl][0][kk], W[sl][1][kk], xa); mf2<true, true, F>(acc[1][0], acc[1][1], W[sl][0][kk], W[sl][1][kk], xb); } \
          mf2<true, true, F>(acc[0][2], acc[0][3], W[10 + sl][0][kk], W[10 + sl][1][kk], xa); mf2<true, true, F>(acc[1][2], acc[1][3], W[10 + sl][0][kk], W[10 + sl][1][kk], xb);
          if (ks == 0) { QKSTEP(true) } else { QKSTEP(false) }
          __builtin_amdgcn_sched_barrier(0);
          xa = na; xb = nb;
        }
        NOPS4(3, acc[0][0], acc[0][1], acc[0][2], acc[0][3]); NOPS4(3, acc[1][0], acc[1][1], acc[1][2], acc[1][3]);
        asm volatile("s_waitcnt vmcnt(0)" ::: "memory");
        const unsigned so = OFF_QK + (s * 32 + h) * 16384 + w * 1024, vo = (gp * 16 + c16) * 16;
#pragma unroll
        for (int mt = 0; mt < 2; ++mt) {
          st16(rs, vo, so + mt * 4096, nosent4(widen4(acc[mt][0], acc[mt][1])));
          st16(rs, vo, so + 8192 + mt * 4096, nosent4(widen4(acc[mt][2], acc[mt][3])));
        }
      } else {
        f32x4 av[2][2];
#pragma unroll
        for (int ks = 0; ks < 20; ++ks) {
          bf16x8 na = xa, nb = xb;
          if (ks + 1 < 20) { na = asb8(ldsr128(lxb + (ks + 1) * 2048)); nb = asb8(ldsr128(lxb + (ks + 1) * 2048 + 1024)); }
          __builtin_amdgcn_sched_barrier(0);
          const int sl = ks >> 1, kk = ks & 1;
#define VSTEP(F) \
          if (sl < NVS) { mf2<false, true, F>(av[0][0], av[0][1], W[sl][0][kk], W[sl][1][kk], xa); mf2<false, true, F>(av[1][0], av[1][1], W[sl][0][kk], W[sl][1][kk], xb); } \
          else { mf2<true, true, F>(av[0][0], av[0][1], W[sl][0][kk], W[sl][1][kk], xa); mf2<true, true, F>(av[1][0], av[1][1], W[sl][0][kk], W[sl][1][kk], xb); }
          if (ks == 0) { VSTEP(true) } else { VSTEP(false) }
          __builtin_amdgcn_sched_barrier(0);
          xa = na; xb = nb;
        }
        NOPS4(7, av[0][0], av[0][1], av[1][0], av[1][1]);
        ldsw128(lv + (w * 2) * 1024 + (gp * 16 + c16) * 16, widen4(av[0][0], av[0][1]));
        ldsw128(lv + (w * 2 + 1) * 1024 + (gp * 16 + c16) * 16, widen4(av[1][0], av[1][1]));
        BAR_LDS();
        bf16x8 vf[4][2];
#pragma unroll
        for (int i = 0; i < 8; ++i) vf[i >> 1][i & 1] = asb8(ldsr128(lv + i * 1024 + lane * 16));
        asm volatile("s_waitcnt vmcnt(0)" ::: "memory");
        __builtin_amdgcn_sched_barrier(0);
        f32x4 cu[5][2][2];
#pragma unroll
        for (int cb = 0; cb < 6; ++cb) {
          if (cb < 5) {
#pragma unroll
            for (int ks = 0; ks < 4; ++ks) {
              const int sl = 10 + 2 * cb + (ks >> 1), kk = ks & 1;
              if (ks == 0) { mf2<true, false, true>(cu[cb][0][0], cu[cb][0][1], W[sl][0][kk], W[sl][1][kk], vf[ks][0]); mf2<true, false, true>(cu[cb][1][0], cu[cb][1][1], W[sl][0][kk], W[sl][1][kk], vf[ks][1]); }
              else { mf2<true, false, false>(cu[cb][0][0], cu[cb][0][1], W[sl][0][kk], W[sl][1][kk], vf[ks][0]); mf2<true, false, false>(cu[cb][1][0], cu[cb][1][1], W[sl][0][kk], W[sl][1][kk], vf[ks][1]); }
            }
          }
          if (cb > 0) {
            f32x4 x0 = cu[cb - 1][0][0], x1 = cu[cb - 1][0][1], y0 = cu[cb - 1][1][0], y1 = cu[cb - 1][1][1];
            if (cb < 5) NOPS4(0, x0, x1, y0, y1); else NOPS4(7, x0, x1, y0, y1);
            const unsigned so = OFF_U + ((s * 40 + (cb - 1) * 8 + 2 * w) * 32 + h) * 1024;
            u32x4 o;
            o[0] = cvtpk(x0[0], x0[1]); o[1] = cvtpk(x0[2], x0[3]); o[2] = cvtpk(y0[0], y0[1]); o[3] = cvtpk(y0[2], y0[3]);
            st16(rs, lane * 16, so, o);
            o[0] = cvtpk(x1[0], x1[1]); o[1] = cvtpk(x1[2], x1[3]); o[2] = cvtpk(y1[0], y1[1]); o[3] = cvtpk(y1[2], y1[3]);
            st16(rs, lane * 16, so + 32768, o);
          }
        }
      }
    }
    BAR_ALL();
    if (t == 0) st4(rs, 0, pword, NSEG);
  }
  if (b >= 168 && b < 208) {
    const int ct = b - 168;
    u32x4 uf[8];
    unsigned seen = 0;
    auto fetch_u = [&](int s) {
      wait_prog(rs, (32 + 8 * w + (lane & 7)) * 256, s + 1, seen, budget);
#pragma unroll
      for (int i = 0; i < 8; ++i) uf[i] = ld16(rs, lane * 16, OFF_U + ((s * 40 + ct) * 32 + 8 * w + i) * 1024);
    };
    for (int s = 0; s < NSEG; ++s) {
      const unsigned ro = OFF_R + s * REC;
      u32x2 xr = {0u, 0u};
      if (w < 2) xr = *(const u32x2*)(x + (size_t)(s * 32 + 16 * w + c16) * D_ + ct * 16 + 4 * g);
      wait_word(rs, ro + R_PT + (8 * w + 7) * 2560 + 1024, budget);
      if (s == 0) fetch_u(0);
      u32x4 v[22];
      poll_n<22>(rs, v,
                 [=](int i) { return i < 16 ? lane * 16 : (i < 20 ? g * 2560 + c16 * 16 : lane * 16); },
                 [=](int i) { return i < 16 ? ro + R_PT + (8 * w + (i >> 1)) * 2560 + (i & 1) * 1024
                                            : (i < 20 ? ro + R_PT + (8 * w + 4 * ((i - 16) >> 1)) * 2560 + 2048 + (i & 1) * 256
                                                      : ro + R_VW + ct * 8192 + (8 * w + 4 * (i - 20)) * 256); }, budget);
      f32x4 a0 = {0.f, 0.f, 0.f, 0.f}, a1 = a0;
#pragma unroll
      for (int i = 0; i < 8; ++i) { a0 = MFMA16(asb8(uf[i]), asb8(v[2 * i]), a0); a1 = MFMA16(asb8(uf[i]), asb8(v[2 * i + 1]), a1); }
#pragma unroll
      for (int i = 0; i < 2; ++i) { a0 = MFMA16(asb8(v[20 + i]), asb8(v[16 + 2 * i]), a0); a1 = MFMA16(asb8(v[20 + i]), asb8(v[17 + 2 * i]), a1); }
      const unsigned lred = l0 + (s & 1) * 8192;
      ldsw128(lred + (w * 2) * 1024 + lane * 16, __builtin_bit_cast(u32x4, a0));
      ldsw128(lred + (w * 2 + 1) * 1024 + lane * 16, __builtin_bit_cast(u32x4, a1));
      BAR_LDS();
      if (w < 2) {
        f32x4 z = __builtin_bit_cast(f32x4, ldsr128(lred + w * 1024 + lane * 16));
#pragma unroll
        for (int i = 1; i < 4; ++i) z += __builtin_bit_cast(f32x4, ldsr128(lred + (i * 2 + w) * 1024 + lane * 16));
        u32x2 o;
        o[0] = nosent(cvtpk(bflo(xr[0]) + rbf(z[0]), bfhi(xr[0]) + rbf(z[1])));
        o[1] = nosent(cvtpk(bflo(xr[1]) + rbf(z[2]), bfhi(xr[1]) + rbf(z[3])));
        st8(rs, c16 * 32 + g * 8, ro + R_H + ct * 1024 + w * 512, o);
      }
      if (s + 1 < NSEG) fetch_u(s + 1);
      fill_share(rs, s, ct, t, ws);
      wait_fill(rs, s, budget);
    }
  } else if ((b >= 208 && b < 235) || (b >= 104 && b < 131)) {
    const int mt2 = b < 208 ? 1 : 0, j = b - (mt2 ? 104 : 208);
    bf16x8 W[20][2];
    load_slabs<10, false, 0>(l0, w, lane, 8 * g + q4, 4, 16 * w + 4 * p4, 64,
                   [=](int sl, int row, int ch) { return Wg + (ch < 8 ? (ptrdiff_t)0 : Wu - Wg) + (size_t)(sl * 64 + row) * F_ + 64 * j + (ch & 7) * 8; },
                   [&](int sl, int tt, bf16x8 lo, bf16x8 hi) { W[2 * sl][tt] = lo; W[2 * sl + 1][tt] = hi; }, [] {});
    const int row = 8 * w + (c16 >> 1), half = lane & 1;
    const unsigned hvo = g * 1024 + row * 32 + half * 16;
    u32x4 v[10];
#pragma unroll
    for (int i = 0; i < 10; ++i) v[i] = u32x4{~0u, ~0u, ~0u, ~0u};
    for (int s = mt2 ? SWG : 0; s < SWG; ++s) {
      const unsigned ro = OFF_R + s * REC;
      unsigned pend = 0;
#pragma unroll
      for (int i = 0; i < 10; ++i) pend |= (PENDING(v[i]) != 0 ? 1u : 0u) << i;
      if (pend == 0x3FFu) wait_word(rs, ro + R_H + w * 256, budget);
      if (pend) poll_n<10>(rs, v, [=](int) { return hvo; }, [=](int i) { return ro + R_H + i * 4096; }, budget, pend);
      ln_fast<10>(v, [](float z) { return xsum32(xsum16(xsum1(z))); });
      const unsigned lh = l0 + (s & 1) * XN_SEG;
#pragma unroll
      for (int i = 0; i < 10; ++i)
        ldsw128(lh + ((2 * i + (g >> 1)) * 2 + (row >> 4)) * 1024 + ((2 * (g & 1) + half) * 16 + (row & 15)) * 16, v[i]);
      wait_fill(rs, s, budget);
      if (s + 1 < SWG) {
#pragma unroll
        for (int i = 0; i < 10; ++i) v[i] = ld16(rs, hvo, ro + REC + R_H + i * 4096);
      }
      BAR_LDS();
      f32x4 acc[2][2];
      bf16x8 xf[20][2];
#pragma unroll
      for (int ks = 0; ks < 4; ++ks) { xf[ks][0] = asb8(ldsr128(lh + ks * 2048 + lane * 16)); xf[ks][1] = asb8(ldsr128(lh + ks * 2048 + 1024 + lane * 16)); }
#pragma unroll
      for (int ks = 0; ks < 20; ++ks) {
        if (ks + 4 < 20) { xf[ks + 4][0] = asb8(ldsr128(lh + (ks + 4) * 2048 + lane * 16)); xf[ks + 4][1] = asb8(ldsr128(lh + (ks + 4) * 2048 + 1024 + lane * 16)); }
        __builtin_amdgcn_sched_barrier(0);
        if (ks == 0) { mf2<true, true, true>(acc[0][0], acc[0][1], W[ks][0], W[ks][1], xf[ks][0]); mf2<true, true, true>(acc[1][0], acc[1][1], W[ks][0], W[ks][1], xf[ks][1]); }
        else { mf2<true, true, false>(acc[0][0], acc[0][1], W[ks][0], W[ks][1], xf[ks][0]); mf2<true, true, false>(acc[1][0], acc[1][1], W[ks][0], W[ks][1], xf[ks][1]); }
        __builtin_amdgcn_sched_barrier(0);
      }
      NOPS4(7, acc[0][0], acc[0][1], acc[1][0], acc[1][1]);
      f32x4 a[2];
#pragma unroll
      for (int mt = 0; mt < 2; ++mt)
#pragma unroll
        for (int i = 0; i < 4; ++i) { const float gt = rbf(acc[mt][0][i]), up = rbf(acc[mt][1][i]); a[mt][i] = (gt > 0.f ? gt : gt * 0.f) * up; }
      st16(rs, (g & 1) * 1024 + ((2 * (w & 1) + (g >> 1)) * 16 + c16) * 16, ro + R_ACT + (2 * j + (w >> 1)) * 2048, nosent4(widen4(a[0], a[1])));
    }
    if (mt2) wait_fill(rs, SWG - 3, budget);
    const int rr = 4 * w + q4;
    const unsigned hvo2 = g * 2048 + (p4 >> 1) * 1024 + (16 * mt2 + rr) * 32 + (p4 & 1) * 16;
    u32x4 u[5];
#pragma unroll
    for (int i = 0; i < 5; ++i) u[i] = u32x4{~0u, ~0u, ~0u, ~0u};
    for (int s = SWG; s < NSEG; ++s) {
      const unsigned ro = OFF_R + s * REC;
      unsigned pend = 0;
#pragma unroll
      for (int i = 0; i < 5; ++i) pend |= (PENDING(u[i]) != 0 ? 1u : 0u) << i;
      if (pend == 0x1Fu) wait_word(rs, ro + R_H + (16 * mt2 + 4 * w) * 32, budget);
      if (pend) poll_n<5>(rs, u, [=](int) { return hvo2; }, [=](int i) { return ro + R_H + i * 8192; }, budget, pend);
      ln_fast<5>(u, [](float z) { return xsum32(xsum16(xsum2(xsum1(z)))); });
      const unsigned lh = l0 + (s & 1) * XN_SEG;
#pragma unroll
      for (int i = 0; i < 5; ++i) ldsw128(lh + (4 * i + g) * 1024 + (p4 * 16 + rr) * 16, u[i]);
      wait_fill(rs, s, budget);
      if (s + 1 < NSEG) {
#pragma unroll
        for (int i = 0; i < 5; ++i) u[i] = ld16(rs, hvo2, ro + REC + R_H + i * 8192);
      }
      BAR_LDS();
      f32x4 ag, au;
      bf16x8 xf[20];
#pragma unroll
      for (int ks = 0; ks < 8; ++ks) xf[ks] = asb8(ldsr128(lh + ks * 1024 + lane * 16));
#pragma unroll
      for (int ks = 0; ks < 20; ++ks) {
        if (ks + 8 < 20) xf[ks + 8] = asb8(ldsr128(lh + (ks + 8) * 1024 + lane * 16));
        __builtin_amdgcn_sched_barrier(0);
        if (ks == 0) mf2<true, true, true>(ag, au, W[ks][0], W[ks][1], xf[ks]); else mf2<true, true, false>(ag, au, W[ks][0], W[ks][1], xf[ks]);
        __builtin_amdgcn_sched_barrier(0);
      }
      NOPS2(7, ag, au);
      float a[4];
#pragma unroll
      for (int i = 0; i < 4; ++i) { const float gt = rbf(ag[i]), up = rbf(au[i]); a[i] = (gt > 0.f ? gt : gt * 0.f) * up; }
      st8(rs, mt2 * 1024 + ((2 * (w & 1) + (g >> 1)) * 16 + c16) * 16 + (g & 1) * 8, ro + R_ACT + (2 * j + (w >> 1)) * 2048,
          u32x2{nosent(cvtpk(a[0], a[1])), nosent(cvtpk(a[2], a[3]))});
    }
  } else if ((b >= 235 && b < 255) || (b >= 136 && b < 156)) {
    const int mt2 = b < 235 ? 1 : 0, d = b - (mt2 ? 136 : 235);
#pragma unroll
    for (int i = 0; i < 27; ++i) glds16(Wd + (size_t)((w * 27 + i) * 16 + (lane >> 2)) * D_ + 32 * d + (lane & 3) * 8, l0 + (w * 27 + i) * 1024);
    BAR_ALL();
    const int k0 = w < 2 ? 14 * w : 13 * w + 2;
    bf16x8 W[14][2];
#pragma unroll
    for (int kk = 0; kk < 14; ++kk)
#pragma unroll
      for (int nt = 0; nt < 2; ++nt) {
        const int ks = (kk == 13 && w >= 2) ? k0 : k0 + kk;
        const unsigned a = l0 + (32 * ks + 8 * g + q4) * 64 + (16 * nt + 4 * p4) * 2;
        W[kk][nt] = cat8(trread(a), trread(a + 256));
      }
    BAR_LDS();
    for (int s = mt2 ? SWD : 0; s < SWD; ++s) {
      const unsigned ro = OFF_R + s * REC;
      wait_word(rs, ro + R_ACT + k0 * 2048, budget);
      u32x4 v[28];
      poll_n<28>(rs, v, [=](int) { return lane * 16; }, [=](int i) { return ro + R_ACT + ((i >= 26 && w >= 2) ? k0 * 2 + (i - 26) : k0 * 2 + i) * 1024; }, budget);
      u32x4 hr = {0u, 0u, 0u, 0u};
      if (w < 2) hr = ld16(rs, (g & 1) * 1024 + c16 * 32 + (g >> 1) * 16, ro + R_H + 2 * d * 1024 + w * 512);
      f32x4 acc[2][2];
#pragma unroll
      for (int kk = 0; kk < 13; ++kk) {
        if (kk == 0) { mf2<true, true, true>(acc[0][0], acc[0][1], W[kk][0], W[kk][1], asb8(v[2 * kk])); mf2<true, true, true>(acc[1][0], acc[1][1], W[kk][0], W[kk][1], asb8(v[2 * kk + 1])); }
        else { mf2<true, true, false>(acc[0][0], acc[0][1], W[kk][0], W[kk][1], asb8(v[2 * kk])); mf2<true, true, false>(acc[1][0], acc[1][1], W[kk][0], W[kk][1], asb8(v[2 * kk + 1])); }
      }
      if (w < 2) { mf2<true, true, false>(acc[0][0], acc[0][1], W[13][0], W[13][1], asb8(v[26])); mf2<true, true, false>(acc[1][0], acc[1][1], W[13][0], W[13][1], asb8(v[27])); }
      NOPS4(7, acc[0][0], acc[0][1], acc[1][0], acc[1][1]);
      const unsigned lred = l0 + (s & 1) * 16384;
#pragma unroll
      for (int i = 0; i < 4; ++i) ldsw128(lred + (w * 4 + i) * 1024 + lane * 16, __builtin_bit_cast(u32x4, acc[i >> 1][i & 1]));
      BAR_LDS();
      if (w < 2) {
        f32x4 z0 = __builtin_bit_cast(f32x4, ldsr128(lred + (2 * w) * 1024 + lane * 16)), z1 = __builtin_bit_cast(f32x4, ldsr128(lred + (2 * w + 1) * 1024 + lane * 16));
#pragma unroll
        for (int i = 1; i < 4; ++i) {
          z0 += __builtin_bit_cast(f32x4, ldsr128(lred + (i * 4 + 2 * w) * 1024 + lane * 16));
          z1 += __builtin_bit_cast(f32x4, ldsr128(lred + (i * 4 + 2 * w + 1) * 1024 + lane * 16));
        }
        const u32x4 o = widen4(z0, z1);
        u32x4 z;
#pragma unroll
        for (int i = 0; i < 4; ++i) z[i] = cvtpk(bflo(hr[i]) + bflo(o[i]), bfhi(hr[i]) + bfhi(o[i]));
        *(u32x4*)(out + (size_t)(s * 32 + 16 * w + c16) * D_ + 32 * d + 16 * (g & 1) + 8 * (g >> 1)) = z;
      }
      fill_share(rs, s, 40 + d, t, ws);
      wait_fill(rs, s, budget);
    }
    if (mt2) wait_fill(rs, SWD - 3, budget);
    for (int s = SWD; s < NSEG; ++s) {
      const unsigned ro = OFF_R + s * REC + mt2 * 1024;
      wait_word(rs, ro + R_ACT + k0 * 2048, budget);
      u32x4 v[14];
      poll_n<14>(rs, v, [=](int) { return lane * 16; }, [=](int i) { return ro + R_ACT + ((i == 13 && w >= 2) ? k0 : k0 + i) * 2048; }, budget);
      u32x4 hr = {0u, 0u, 0u, 0u};
      if (w == 0) hr = ld16(rs, (g & 1) * 1024 + c16 * 32 + (g >> 1) * 16, ro + R_H + 2 * d * 1024 - mt2 * 512);
      f32x4 a0, a1;
#pragma unroll
      for (int kk = 0; kk < 13; ++kk) {
        if (kk == 0) mf2<true, true, true>(a0, a1, W[kk][0], W[kk][1], asb8(v[kk])); else mf2<true, true, false>(a0, a1, W[kk][0], W[kk][1], asb8(v[kk]));
      }
      if (w < 2) mf2<true, true, false>(a0, a1, W[13][0], W[13][1], asb8(v[13]));
      NOPS2(7, a0, a1);
      const unsigned lred = l0 + (s & 1) * 16384;
      ldsw128(lred + (w * 2) * 1024 + lane * 16, __builtin_bit_cast(u32x4, a0));
      ldsw128(lred + (w * 2 + 1) * 1024 + lane * 16, __builtin_bit_cast(u32x4, a1));
      BAR_LDS();
      if (w == 0) {
        f32x4 z0 = __builtin_bit_cast(f32x4, ldsr128(lred + lane * 16)), z1 = __builtin_bit_cast(f32x4, ldsr128(lred + 1024 + lane * 16));
#pragma unroll
        for (int i = 1; i < 4; ++i) {
          z0 += __builtin_bit_cast(f32x4, ldsr128(lred + (i * 2) * 1024 + lane * 16));
          z1 += __builtin_bit_cast(f32x4, ldsr128(lred + (i * 2 + 1) * 1024 + lane * 16));
        }
        const u32x4 o = widen4(z0, z1);
        u32x4 z;
#pragma unroll
        for (int i = 0; i < 4; ++i) z[i] = cvtpk(bflo(hr[i]) + bflo(o[i]), bfhi(hr[i]) + bfhi(o[i]));
        *(u32x4*)(out + (size_t)(s * 32 + 16 * mt2 + c16) * D_ + 32 * d + 16 * (g & 1) + 8 * (g >> 1)) = z;
      }
      wait_fill(rs, s, budget);
    }
  }
}

extern "C" void kernel_launch(void* const* d_in, const int* in_sizes, int n_in, void* d_out, int out_size, void* d_ws, size_t ws_size,
                              hipStream_t stream) {
  const u16* x = (const u16*)d_in[0];
  char* ws = (char*)d_ws;
  prep_kernel<<<128 + FILL_BLOCKS + 1, 256, 0, stream>>>(x, (const u16*)d_in[1], ws);
  mega_kernel<<<256, 256, 0, stream>>>(x, (const u16*)d_in[2], (const u16*)d_in[3], (const u16*)d_in[4], (const u16*)d_in[5],
                                       (const u16*)d_in[8], (const u16*)d_in[9], (const u16*)d_in[10], (u16*)d_out, ws);
}
```

```cpp
#include <hip/hip_runtime.h>
#include <hip/hip_bf16.h>
#include <stdint.h>

typedef __bf16 bf16x8 __attribute__((ext_vector_type(8)));
typedef __bf16 bf16x16 __attribute__((ext_vector_type(16)));
typedef __bf16 bf16x4 __attribute__((ext_vector_type(4)));
typedef float f32x4 __attribute__((ext_vector_type(4)));
typedef unsigned u32x4 __attribute__((ext_vector_type(4)));
typedef unsigned u32x2 __attribute__((ext_vector_type(2)));
typedef short s16x4 __attribute__((ext_vector_type(4)));
typedef __attribute__((address_space(3))) char* lds_ptr;
typedef unsigned short u16;

#define DEVI __device__ __forceinline__

constexpr int S_ = 2048, D_ = 640, HE = 4096, F_ = 1728, NSEG = 64;
constexpr int NPRE = 8;
constexpr int NFILL = 60;
constexpr unsigned XN_SEG = 40960, QK_SEG = 32 * 16384, U_SEG = 40 * 32 * 1024;
constexpr unsigned OFF_XN = 0;
constexpr unsigned OFF_QK = OFF_XN + NSEG * XN_SEG;
constexpr unsigned OFF_U = OFF_QK + NSEG * QK_SEG;
constexpr unsigned OFF_FLG = OFF_U + NSEG * U_SEG;
constexpr unsigned OFF_R = OFF_FLG + 72 * 256;
#define RM 8
#define RP 8
constexpr unsigned R_MEM = 0, R_P = RM * 10240, R_VW = R_P + RP * 20480, R_PT = R_VW + 327680, R_H = R_PT + 32 * 2560, R_ACT = R_H + 40960,
                   REC = R_ACT + 54 * 2048;
constexpr unsigned OFF_END = OFF_R + NSEG * REC;
static_assert(OFF_R % 256 == 0 && REC % 256 == 0 && OFF_U % 256 == 0, "align");
static_assert((size_t)OFF_END + (4u << 20) <= (size_t)256 * 1024 * 1024, "ws");

typedef __bf16 bf16x2 __attribute__((ext_vector_type(2)));
typedef float f32x2 __attribute__((ext_vector_type(2)));
DEVI unsigned cvtpk(float lo, float hi) { const f32x2 v = {lo, hi}; return __builtin_bit_cast(unsigned, __builtin_convertvector(v, bf16x2)); }
DEVI float bflo(unsigned u) { return __uint_as_float(u << 16); }
DEVI float bfhi(unsigned u) { return __uint_as_float(u & 0xffff0000u); }
DEVI float rbf(float x) { return bflo(cvtpk(x, x)); }
DEVI unsigned nosent(unsigned u) { return u < 0xFFFFFFFEu ? u : 0xFFFFFFFEu; }
DEVI void glds16(const void* g, unsigned lds_base) {
  unsigned sv;
  asm volatile("s_mov_b32 %0, m0\n\ts_mov_b32 m0, %2\n\ts_nop 0\n\tglobal_load_lds_dwordx4 %1, off\n\ts_mov_b32 m0, %0" : "=&s"(sv) : "v"(g), "s"(lds_base) : "memory");
}
DEVI s16x4 trread(unsigned a) { return __builtin_amdgcn_ds_read_tr16_b64_v4i16((__attribute__((address_space(3))) s16x4*)(uintptr_t)a); }
DEVI bf16x8 cat8(s16x4 a, s16x4 b) { return __builtin_bit_cast(bf16x8, __builtin_shufflevector(a, b, 0, 1, 2, 3, 4, 5, 6, 7)); }
DEVI u32x4 ldsr128(unsigned a) { return *(__attribute__((address_space(3))) u32x4*)(uintptr_t)a; }
DEVI u32x4 ldsr128o(unsigned a) { return *(volatile __attribute__((address_space(3))) u32x4*)(uintptr_t)a; }
DEVI u32x2 ldsr64(unsigned a) { return *(__attribute__((address_space(3))) u32x2*)(uintptr_t)a; }
DEVI void ldsw128(unsigned a, u32x4 v) { *(__attribute__((address_space(3))) u32x4*)(uintptr_t)a = v; }
DEVI void ldsw64(unsigned a, u32x2 v) { *(__attribute__((address_space(3))) u32x2*)(uintptr_t)a = v; }
DEVI bf16x8 asb8(u32x4 v) { return __builtin_bit_cast(bf16x8, v); }
#define MFMA16(a, b, c) __builtin_amdgcn_mfma_f32_16x16x32_bf16(a, b, c, 0, 0, 0)
#define SMF(a, b, c, idx) __builtin_amdgcn_smfmac_f32_16x16x64_bf16(a, b, c, idx, 0, 0)
#define BAR_ALL() asm volatile("s_waitcnt vmcnt(0) lgkmcnt(0)\n\ts_barrier" ::: "memory")
#define BAR_LDS() asm volatile("s_waitcnt lgkmcnt(0)\n\ts_barrier" ::: "memory")
DEVI float xmax16(float x) { auto r = __builtin_amdgcn_permlane16_swap(__float_as_uint(x), __float_as_uint(x), false, false); return fmaxf(__uint_as_float(r[0]), __uint_as_float(r[1])); }
DEVI float xmax32(float x) { auto r = __builtin_amdgcn_permlane32_swap(__float_as_uint(x), __float_as_uint(x), false, false); return fmaxf(__uint_as_float(r[0]), __uint_as_float(r[1])); }
DEVI float xsum16(float x) { auto r = __builtin_amdgcn_permlane16_swap(__float_as_uint(x), __float_as_uint(x), false, false); return __uint_as_float(r[0]) + __uint_as_float(r[1]); }
DEVI float xsum32(float x) { auto r = __builtin_amdgcn_permlane32_swap(__float_as_uint(x), __float_as_uint(x), false, false); return __uint_as_float(r[0]) + __uint_as_float(r[1]); }
DEVI float pairsum32(float a, float b) { auto r = __builtin_amdgcn_permlane32_swap(__float_as_uint(a), __float_as_uint(b), false, false); return __uint_as_float(r[0]) + __uint_as_float(r[1]); }
DEVI void st_sc1_x4(void* p, u32x4 v) { asm volatile("global_store_dwordx4 %0, %1, off sc1\n\ts_nop 1" ::"v"(p), "v"(v) : "memory"); }
DEVI void st_sc1_x2(void* p, u32x2 v) { asm volatile("global_store_dwordx2 %0, %1, off sc1\n\ts_nop 1" ::"v"(p), "v"(v) : "memory"); }
DEVI void st_sc1_x1(void* p, unsigned v) { asm volatile("global_store_dword %0, %1, off sc1\n\ts_nop 1" ::"v"(p), "v"(v) : "memory"); }
DEVI unsigned umax4(u32x4 v) { return max(max(v[0], v[1]), max(v[2], v[3])); }

typedef unsigned long long u64;
typedef __amdgpu_buffer_rsrc_t rsrc_t;
DEVI u32x4 ld16(rsrc_t rs, unsigned v, unsigned s) { return __builtin_amdgcn_raw_buffer_load_b128(rs, v, s, 16); }
DEVI unsigned ld4(rsrc_t rs, unsigned v, unsigned s) { return __builtin_amdgcn_raw_buffer_load_b32(rs, v, s, 16); }
DEVI void st16(rsrc_t rs, unsigned v, unsigned s, u32x4 d) { __builtin_amdgcn_raw_buffer_store_b128(d, rs, v, s, 16); }
DEVI void st8(rsrc_t rs, unsigned v, unsigned s, u32x2 d) { __builtin_amdgcn_raw_buffer_store_b64(d, rs, v, s, 16); }
DEVI void st4(rsrc_t rs, unsigned v, unsigned s, unsigned d) { __builtin_amdgcn_raw_buffer_store_b32(d, rs, v, s, 16); }
#define SB() __builtin_amdgcn_sched_barrier(0)
DEVI void touch(rsrc_t rs, unsigned v, unsigned s) { const unsigned x = ld4(rs, v, s); asm volatile("" :: "v"(x)); }
DEVI unsigned ror8(unsigned x) { return __builtin_amdgcn_mov_dpp(x, 0x128, 0xF, 0xF, true); }
DEVI float xmax8(float x) { return fmaxf(x, __uint_as_float(ror8(__float_as_uint(x)))); }
DEVI float xsum8(float x) { return x + __uint_as_float(ror8(__float_as_uint(x))); }
#define RED8(OP, x, y) do { float t_; asm volatile(OP " %0, %0, %2\n\ts_nop 1\n\t" OP "_dpp %0, %0, %0 row_ror:8 row_mask:0xf bank_mask:0xf\n\tv_mov_b32 %1, %0\n\ts_nop 1\n\t" \
    "v_permlane16_swap_b32 %0, %1\n\t" OP " %0, %0, %1\n\tv_mov_b32 %1, %0\n\ts_nop 1\n\tv_permlane32_swap_b32 %0, %1\n\t" OP " %0, %0, %1" : "+v"(x), "=&v"(t_) : "v"(y)); } while (0)
DEVI float vmax3(float a, float b, float c) { float r; asm("v_max3_f32 %0, %1, %2, %3" : "=v"(r) : "v"(a), "v"(b), "v"(c)); return r; }
DEVI f32x4 pairsum4(f32x4 a, f32x4 b) {
  u32x4 x, y;
#pragma unroll
  for (int i = 0; i < 4; ++i) { auto r = __builtin_amdgcn_permlane32_swap(__float_as_uint(a[i]), __float_as_uint(b[i]), false, false); x[i] = r[0]; y[i] = r[1]; }
  return __builtin_bit_cast(f32x4, x) + __builtin_bit_cast(f32x4, y);
}
#define NOMOVE() asm volatile("" ::: "memory")
#define PENDING(v) __builtin_amdgcn_ballot_w64(umax4(v) == ~0u)
DEVI float xsum2(float x) { return x + __uint_as_float(__builtin_amdgcn_update_dpp(0u, __float_as_uint(x), 0x4E, 0xF, 0xF, true)); }
DEVI float xsum1(float x) { return x + __uint_as_float(__builtin_amdgcn_update_dpp(0u, __float_as_uint(x), 0xB1, 0xF, 0xF, true)); }
DEVI u32x4 widen(unsigned a0, unsigned a1, unsigned b0, unsigned b1) {
  auto r0 = __builtin_amdgcn_permlane16_swap(a0, b0, false, false);
  auto r1 = __builtin_amdgcn_permlane16_swap(a1, b1, false, false);
  u32x4 o; o[0] = r0[0]; o[1] = r1[0]; o[2] = r0[1]; o[3] = r1[1]; return o;
}
DEVI u32x4 widen4(f32x4 x, f32x4 y) { return widen(cvtpk(x[0], x[1]), cvtpk(x[2], x[3]), cvtpk(y[0], y[1]), cvtpk(y[2], y[3])); }
DEVI u32x4 nosent4(u32x4 v) { u32x4 o; o[0] = nosent(v[0]); o[1] = nosent(v[1]); o[2] = nosent(v[2]); o[3] = nosent(v[3]); return o; }

template <int N, class RED>
DEVI void ln_apply(u32x4 (&v)[N], RED red) {
  float s = 0.f;
#pragma unroll
  for (int i = 0; i < N; ++i)
#pragma unroll
    for (int j = 0; j < 4; ++j) s += bflo(v[i][j]) + bfhi(v[i][j]);
  const float mean = red(s) * (1.0f / 640.0f);
  float q = 0.f;
#pragma unroll
  for (int i = 0; i < N; ++i)
#pragma unroll
    for (int j = 0; j < 4; ++j) { const float a = bflo(v[i][j]) - mean, b = bfhi(v[i][j]) - mean; q += a * a + b * b; }
  const float mb = rbf(mean), vb = rbf(red(q) * (1.0f / 640.0f));
  const float sd = rbf(sqrtf(rbf(vb + 1e-05f)));
#pragma unroll
  for (int i = 0; i < N; ++i)
#pragma unroll
    for (int j = 0; j < 4; ++j) v[i][j] = cvtpk(rbf(bflo(v[i][j]) - mb) / sd, rbf(bfhi(v[i][j]) - mb) / sd);
}

template <int N, class RED>
DEVI void ln_fast(u32x4 (&v)[N], RED red) {
  f32x2 s2 = {0.f, 0.f};
#pragma unroll
  for (int i = 0; i < N; ++i)
#pragma unroll
    for (int j = 0; j < 4; ++j) s2 += f32x2{bflo(v[i][j]), bfhi(v[i][j])};
  const float mean = red(s2[0] + s2[1]) * (1.0f / 640.0f);
  const float mb = rbf(mean);
  const f32x2 m2 = {mb, mb};
  f32x2 q2 = {0.f, 0.f};
#pragma unroll
  for (int i = 0; i < N; ++i)
#pragma unroll
    for (int j = 0; j < 4; ++j) { const f32x2 d = f32x2{bflo(v[i][j]), bfhi(v[i][j])} - m2; q2 += d * d; v[i][j] = cvtpk(d[0], d[1]); }
  const float dm = mean - mb;
  const float vb = rbf(fmaxf(red(q2[0] + q2[1]) * (1.0f / 640.0f) - dm * dm, 0.f));
  const float r = 1.0f / rbf(sqrtf(rbf(vb + 1e-05f)));
  const f32x2 r2 = {r, r};
#pragma unroll
  for (int i = 0; i < N; ++i)
#pragma unroll
    for (int j = 0; j < 4; ++j) { const f32x2 d = f32x2{bflo(v[i][j]), bfhi(v[i][j])} * r2; v[i][j] = cvtpk(d[0], d[1]); }
}

constexpr int FILL_BLOCKS = 256;
__global__ __launch_bounds__(256) void prep_kernel(const u16* __restrict__ x, const u16* __restrict__ mem0, char* __restrict__ ws) {
  const int b = blockIdx.x, t = threadIdx.x;
  if (b < 128) {
    const int row = b * 16 + (t >> 4), i = t & 15;
    u32x4 v[5];
#pragma unroll
    for (int j = 0; j < 5; ++j) v[j] = *(const u32x4*)(x + (size_t)row * D_ + (i + 16 * j) * 8);
    ln_apply<5>(v, [](float s) { s += __shfl_xor(s, 1); s += __shfl_xor(s, 2); s += __shfl_xor(s, 4); s += __shfl_xor(s, 8); return s; });
    char* dst = ws + OFF_XN + (size_t)(row >> 5) * XN_SEG + ((row >> 4) & 1) * 1024 + ((i & 3) * 16 + (row & 15)) * 16;
#pragma unroll
    for (int j = 0; j < 5; ++j) *(u32x4*)(dst + (4 * j + (i >> 2)) * 2048) = v[j];
  } else if (b < 128 + FILL_BLOCKS) {
    u32x4* p = (u32x4*)(ws + OFF_R + R_P);
    const unsigned n = (NPRE * REC - R_P) / 16;
    const u32x4 s = {~0u, ~0u, ~0u, ~0u};
    for (unsigned i = (b - 128) * 256 + t; i < n; i += FILL_BLOCKS * 256) p[i] = s;
    if ((b - 128) * 256 + t < QK_SEG / 16) ((u32x4*)(ws + OFF_QK))[(b - 128) * 256 + t] = s;
  } else {
    if (t < 72) ((unsigned*)(ws + OFF_FLG))[t * 64] = 0u;
    for (int i = t; i < 640; i += 256) {
      const int ct = i >> 4, bb = (i >> 3) & 1, r = i & 7;
      const u32x4 m = nosent4(*(const u32x4*)(mem0 + r * 640 + ct * 16 + bb * 8));
#pragma unroll
      for (int j = 0; j < RM; ++j) *(u32x4*)(ws + OFF_R + R_MEM + j * 10240 + (size_t)i * 16) = m;
    }
  }
}

constexpr float SCALE = 0.08838834764831845f, LOG2E = 1.4426950408889634f;
constexpr unsigned SPIN_BUDGET = 1u << 22;
constexpr int CH_STG = 65536;
constexpr int CH_MEM = 0, CH_X = CH_MEM + 10240, CH_LDS = 2 * CH_STG;

#define PSLP 1
#define PCAP 6
#define PCAPC 49
#define STR2(x) #x
#define STR(x) STR2(x)
#define CLOB3 "v244", "v245", "v246", "v247", "v248", "v249", "v250", "v251", "v252", "v253", "v254", "v255"
#define CLOB7 "v220", "v221", "v222", "v223", "v224", "v225", "v226", "v227", "v228", "v229", "v230", "v231", "v232", "v233", "v234", "v235", "v236", "v237", "v238", "v239", "v240", "v241", "v242", "v243", CLOB3
#define PEND_ASM(t, m, r0, r1, r2, r3) "v_max_u32 " t ", v" #r2 ", v" #r3 "\n\tv_max3_u32 " t ", v" #r0 ", v" #r1 ", " t "\n\tv_cmp_eq_u32 " m ", -1, " t "\n\t"
#define CHK3 "s_sleep " STR(PSLP) "\n\t" PEND_ASM("%[t]", "%[m0]", 244, 245, 246, 247) PEND_ASM("%[t]", "%[m1]", 248, 249, 250, 251) \
             PEND_ASM("%[t]", "%[m2]", 252, 253, 254, 255) "s_or_b64 %[x], %[m0], %[m1]\n\ts_or_b64 %[x], %[x], %[m2]\n\ts_cbranch_scc0 .Lpd%=\n\t"
#define CHK3N CHK3 CHK3 CHK3
DEVI void poll_mem(const char* memp, unsigned lmem, int w, int lane, unsigned& budget) {
  const char* p0 = memp + w * 2560 + lane * 16;
  const unsigned la = lmem + w * 2560 + lane * 16;
  u64 m0, m1, m2, x, sv; unsigned t, bud = __builtin_amdgcn_readfirstlane(budget);
  asm volatile("s_mov_b64 %[sv], exec\n\ts_mov_b64 %[m0], -1\n\ts_mov_b64 %[m1], -1\n\ts_mov_b64 %[m2], 0xffffffff\n"
               ".Lpl%=:\n\ts_waitcnt vmcnt(" STR(PCAP) ")\n\t"
               "s_mov_b64 exec, %[m0]\n\tglobal_load_dwordx4 v[244:247], %[p0], off sc1\n\t"
               "s_mov_b64 exec, %[m1]\n\tglobal_load_dwordx4 v[248:251], %[p0], off offset:1024 sc1\n\t"
               "s_mov_b64 exec, %[m2]\n\tglobal_load_dwordx4 v[252:255], %[p0], off offset:2048 sc1\n\t"
               "s_mov_b64 exec, %[sv]\n\t"
               CHK3N
               "s_sub_u32 %[bud], %[bud], 1\n\ts_cbranch_scc0 .Lpl%=\n\ts_mov_b32 %[bud], 0\n"
               ".Lpd%=:\n\t"
               "ds_write_b128 %[la], v[244:247]\n\tds_write_b128 %[la], v[248:251] offset:1024\n\t"
               "s_mov_b64 exec, 0xffffffff\n\tds_write_b128 %[la], v[252:255] offset:2048\n\ts_mov_b64 exec, %[sv]\n\t"
               "s_waitcnt lgkmcnt(0)\n\ts_barrier"
               : [m0] "=&s"(m0), [m1] "=&s"(m1), [m2] "=&s"(m2), [x] "=&s"(x), [sv] "=&s"(sv), [t] "=&v"(t), [bud] "+s"(bud)
               : [p0] "v"(p0), [la] "v"(la) : "memory", "scc", CLOB3);
  budget = bud;
}
#define VM0() __builtin_amdgcn_s_waitcnt(0x0F70)
DEVI void drain_mem(int lane) {
  const unsigned z = lane < 32 ? ~0u : 0u;
  VM0();
  asm volatile("s_waitcnt vmcnt(0)\n\tv_mov_b32 v244, -1\n\tv_mov_b32 v245, -1\n\tv_mov_b32 v246, -1\n\tv_mov_b32 v247, -1\n\t"
               "v_mov_b32 v248, -1\n\tv_mov_b32 v249, -1\n\tv_mov_b32 v250, -1\n\tv_mov_b32 v251, -1\n\t"
               "v_mov_b32 v252, %0\n\tv_mov_b32 v253, %0\n\tv_mov_b32 v254, %0\n\tv_mov_b32 v255, %0" :: "v"(z) : "memory", CLOB3);
}
DEVI void wait_prog(rsrc_t rs, unsigned voff, unsigned need, unsigned& seen, unsigned& budget) {
  while (__builtin_amdgcn_ballot_w64(seen < need) != 0 && budget != 0) {
    NOMOVE();
    seen = ld4(rs, voff, OFF_FLG);
    --budget;
    if (__builtin_amdgcn_ballot_w64(seen < need) != 0) __builtin_amdgcn_s_sleep(8);
  }
  NOMOVE();
}
DEVI void wait_fill(rsrc_t rs, int s, unsigned& budget) {
  if ((s & (NPRE - 1)) == NPRE - 3 && s + NPRE < NSEG) { unsigned seen = 0; wait_prog(rs, (64 + s / NPRE) * 256, NFILL, seen, budget); }
}
DEVI void wait_word(rsrc_t rs, unsigned soff, unsigned& budget) {
  for (;;) {
    NOMOVE();
    const unsigned v = ld4(rs, 0, soff);
    if (__builtin_amdgcn_readfirstlane(v) != ~0u || budget == 0) break;
    --budget;
    __builtin_amdgcn_s_sleep(2);
  }
  NOMOVE();
}
template <int N, class OFFV, class OFFS>
DEVI void poll_n(rsrc_t rs, u32x4 (&v)[N], OFFV offv, OFFS offs, unsigned& budget, unsigned pend = (1u << N) - 1u) {
  for (;;) {
    NOMOVE();
#pragma unroll
    for (int i = 0; i < N; ++i) if (pend & (1u << i)) v[i] = ld16(rs, offv(i), offs(i));
    unsigned np = 0;
#pragma unroll
    for (int i = 0; i < N; ++i) np |= (PENDING(v[i]) != 0 ? 1u : 0u) << i;
    pend = np;
    if (pend == 0 || budget == 0) break;
    --budget;
  }
  NOMOVE();
}
template <bool AG>
DEVI void smf2(f32x4& c0, f32x4& c1, bf16x8 a, const bf16x16& w0, const bf16x16& w1, int idx) {
  if (AG) asm volatile("v_smfmac_f32_16x16x64_bf16 %0, %2, %3, %5\n\tv_smfmac_f32_16x16x64_bf16 %1, %2, %4, %5" : "+v"(c0), "+v"(c1) : "v"(a), "a"(w0), "a"(w1), "v"(idx));
  else asm volatile("v_smfmac_f32_16x16x64_bf16 %0, %2, %3, %5\n\tv_smfmac_f32_16x16x64_bf16 %1, %2, %4, %5" : "+v"(c0), "+v"(c1) : "v"(a), "v"(w0), "v"(w1), "v"(idx));
}
template <bool AG, bool WA, bool FIRST>
DEVI void mf2(f32x4& c0, f32x4& c1, const bf16x8& w0, const bf16x8& w1, bf16x8 x) {
#define MF2_ASM(T, CO, WC) asm volatile(T : CO(c0), CO(c1) : WC(w0), WC(w1), "v"(x))
#define T_WA_F "v_mfma_f32_16x16x32_bf16 %0, %2, %4, 0\n\tv_mfma_f32_16x16x32_bf16 %1, %3, %4, 0"
#define T_WA_N "v_mfma_f32_16x16x32_bf16 %0, %2, %4, %0\n\tv_mfma_f32_16x16x32_bf16 %1, %3, %4, %1"
#define T_WB_F "v_mfma_f32_16x16x32_bf16 %0, %4, %2, 0\n\tv_mfma_f32_16x16x32_bf16 %1, %4, %3, 0"
#define T_WB_N "v_mfma_f32_16x16x32_bf16 %0, %4, %2, %0\n\tv_mfma_f32_16x16x32_bf16 %1, %4, %3, %1"
  if (AG) {
    if (WA) { if (FIRST) MF2_ASM(T_WA_F, "=&v", "a"); else MF2_ASM(T_WA_N, "+v", "a"); }
    else { if (FIRST) MF2_ASM(T_WB_F, "=&v", "a"); else MF2_ASM(T_WB_N, "+v", "a"); }
  } else {
    if (WA) { if (FIRST) MF2_ASM(T_WA_F, "=&v", "v"); else MF2_ASM(T_WA_N, "+v", "v"); }
    else { if (FIRST) MF2_ASM(T_WB_F, "=&v", "v"); else MF2_ASM(T_WB_N, "+v", "v"); }
  }
}
template <bool FIRST>
DEVI void mf1(f32x4& c, u32x4 a, bf16x8 b) {
  if (FIRST) asm volatile("v_mfma_f32_16x16x32_bf16 %0, %1, %2, 0" : "=&v"(c) : "v"(a), "v"(b));
  else asm volatile("v_mfma_f32_16x16x32_bf16 %0, %1, %2, %0" : "+v"(c) : "v"(a), "v"(b));
}
#define DLY_CU 60
#define SWG 40
#define SWD 48
#define DA 162
#define DB 187
#define DC 200
#define PADDC 12
#define PNOM 312
struct Pace {
  unsigned last, per, until;
  DEVI void mark(int d, int more = 0) {
    const unsigned now = (unsigned)__builtin_amdgcn_s_memrealtime(), p = now - last;
    until = now + (unsigned)max(d + min((int)min(min(p, per), 1000u) - PNOM, more), 0);
    last = now; per = p;
  }
  DEVI void pause() const { while ((int)((unsigned)__builtin_amdgcn_s_memrealtime() - until) < 0) __builtin_amdgcn_s_sleep(1); }
};
DEVI void delay_ticks(unsigned n) { const u64 t = __builtin_amdgcn_s_memrealtime() + n; while (__builtin_amdgcn_s_memrealtime() < t) __builtin_amdgcn_s_sleep(4); }
#define NOPS2(n, c0, c1) asm volatile("s_nop " #n : "+v"(c0), "+v"(c1))
#define NOPS4(n, c0, c1, c2, c3) asm volatile("s_nop " #n : "+v"(c0), "+v"(c1), "+v"(c2), "+v"(c3))
constexpr int NVS = 5;

template <bool SP> DEVI int swz(int row) { return SP ? (((row & 1) << 1) | (row & 12)) : (((row & 3) << 2) | ((row >> 2) & 3)); }
template <int NSL, bool SP, int NMID, class SRC, class PUT, class MID>
DEVI void load_slabs(unsigned l0, int w, int lane, int rbase, int rstep, int col0, int colt, SRC src, PUT put, MID mid) {
  constexpr int NB = (NSL + 3) / 4;
  auto stage = [&](int bt) {
#pragma unroll
    for (int j = 0; j < 16; ++j) {
      const int sl = bt * 4 + (j >> 2), blk = w * 4 + (j & 3), pos = blk * 64 + lane;
      if (sl < NSL) glds16(src(sl, pos >> 4, (pos & 15) ^ swz<SP>(pos >> 4)), l0 + (bt & 1) * CH_STG + (j >> 2) * 16384 + blk * 1024);
    }
  };
  stage(0);
#pragma unroll
  for (int bt = 0; bt < NB; ++bt) {
    if (bt + 1 < NB) {
      stage(bt + 1);
      if ((bt + 2) * 4 <= NSL) asm volatile("s_waitcnt vmcnt(16)\n\ts_barrier" ::: "memory");
      else asm volatile("s_waitcnt vmcnt(8)\n\ts_barrier" ::: "memory");
    } else asm volatile("s_waitcnt vmcnt(%0)\n\ts_barrier" :: "n"(NMID) : "memory");
#pragma unroll
    for (int j = 0; j < 4; ++j)
#pragma unroll
      for (int tt = 0; tt < 2; ++tt) {
        if (bt * 4 + j < NSL) {
          s16x4 r[4];
#pragma unroll
          for (int i = 0; i < 4; ++i) {
            const int row = 32 * (i >> 1) + rbase + rstep * (i & 1), cb = (col0 + colt * tt) * 2;
            r[i] = trread(l0 + (bt & 1) * CH_STG + j * 16384 + row * 256 + ((((cb >> 4) ^ swz<SP>(row)) << 4) | (cb & 15)));
          }
          put(bt * 4 + j, tt, cat8(r[0], r[1]), cat8(r[2], r[3]));
        }
      }
    BAR_LDS();
    if (bt + 2 == NB) mid();
  }
}
DEVI const u16* head_src(const u16* p0, const u16* p1, bool qk, int h, int sl, int row, int ch) {
  if (sl < 10) return p0 + (size_t)(sl * 64 + row) * HE + h * 128 + ch * 8;
  const int o = qk ? (sl - 10) * 64 * HE + h * 128 : (h * 128 + ((sl - 10) & 1) * 64) * D_ + ((sl - 10) >> 1) * 128;
  return p1 + o + row * (qk ? HE : D_) + ch * 8;
}
DEVI void fill_share(rsrc_t rs, int s, int idx, int t, char* ws) {
  if ((s & 3) != 0 || (s / 4 + 1) * NPRE >= NSEG) return;
  const int c = s / 4;
  const u32x4 sv = {~0u, ~0u, ~0u, ~0u};
  constexpr unsigned n = NPRE * REC / 4096;
  static_assert(NPRE * REC % 4096 == 0, "fill");
  for (unsigned i = idx; i < n; i += NFILL) st16(rs, t * 16, OFF_R + (c + 1) * NPRE * REC + i * 4096, sv);
  BAR_ALL();
  if (t == 0) __hip_atomic_fetch_add((unsigned*)(ws + OFF_FLG) + (64 + c) * 64, 1u, __ATOMIC_RELAXED, __HIP_MEMORY_SCOPE_AGENT);
}

__global__ __launch_bounds__(256, 1) void mega_kernel(const u16* __restrict__ x, const u16* __restrict__ Wq, const u16* __restrict__ Wk,
                                                       const u16* __restrict__ Wv, const u16* __restrict__ Wo, const u16* __restrict__ Wg,
                                                       const u16* __restrict__ Wu, const u16* __restrict__ Wd, u16* __restrict__ out, char* ws) {
  __shared__ __attribute__((aligned(16))) char smem[CH_LDS];
  const unsigned l0 = (unsigned)(uintptr_t)(lds_ptr)smem;
  const int t = threadIdx.x, lane = t & 63, w = __builtin_amdgcn_readfirstlane(t >> 6);
  const int b = blockIdx.x;
  unsigned budget = SPIN_BUDGET;
  Pace pace = {0u, 0u, 0u};
  const rsrc_t rs = __builtin_amdgcn_make_buffer_rsrc((void*)ws, 0, 0x10000000, 0x00020000);
  char* const rec0 = ws + OFF_R;
  const int g = lane >> 4, c16 = lane & 15, q4 = c16 >> 2, p4 = lane & 3;
  const int gp = 2 * (g & 1) + (g >> 1);
  if (b < 64) {
    const bool roleA = b < 32;
    const int h = b & 31;
    bf16x16 W[20][2];
    const u16* const p0 = roleA ? Wq : Wv; const u16* const p1 = roleA ? Wk : Wo;
    load_slabs<20, true, 0>(l0, w, lane, 16 * (g >> 1) + 4 * (g & 1) + (q4 & 1) + 8 * (q4 >> 1), 2, 32 * w + 4 * p4, 16,
                   [=](int sl, int row, int ch) { return head_src(p0, p1, roleA, h, sl, row, ch); },
                   [&](int sl, int tt, bf16x8 lo, bf16x8 hi) { W[sl][tt] = __builtin_shufflevector(lo, hi, 0, 1, 2, 3, 4, 5, 6, 7, 8, 9, 10, 11, 12, 13, 14, 15); }, [] {});
    const int sidx = (lane & 8) ? 0xEEEE : 0x4444;
    const unsigned lmem = l0 + CH_MEM, lx = l0 + CH_X;
    drain_mem(lane);

    if (roleA) {
      u32x4 kq[12] = {};
      unsigned seen = 0;
      const unsigned qoff = w == 2 ? 4096 : 0;
      auto fetch_qk = [&](int s) {
        wait_prog(rs, h * 256, s + 1, seen, budget);
        const unsigned so = OFF_QK + (s * 32 + h) * 16384;
#pragma unroll
        for (int i = 0; i < 12; ++i) kq[i] = ld16(rs, lane * 16, so + (i < 8 ? 8192 + i * 1024 : qoff + (i - 8) * 1024));
      };
      const int lim = 16 * (w - 1) + c16;
      const unsigned xw = (32 * w + 16 * (lane >> 5) + c16) * 16 + (g & 1) * 8, xr = (8 * g + q4) * 16 + (p4 & 1) * 8;
      VM0();
      for (int s = 0; s < NSEG; ++s) {
        char* const rec = rec0 + (size_t)s * REC;
        f32x4 aq0 = {0.f, 0.f, 0.f, 0.f}, aq1 = aq0, ak0 = aq0, ak1 = aq0;
        NOPS2(0, aq0, aq1); NOPS2(0, ak0, ak1);
        poll_mem(rec + R_MEM + (b % RM) * 10240, lmem, w, lane, budget);
        bf16x8 af[10];
#pragma unroll
        for (int ks = 0; ks < 10; ++ks) af[ks] = asb8(ldsr128o(lmem + ks * 1024 + lane * 16));
        SB();
#pragma unroll
        for (int ks = 0; ks < 10; ++ks) {
          if (ks < NVS) smf2<false>(aq0, aq1, af[ks], W[ks][0], W[ks][1], sidx); else smf2<true>(aq0, aq1, af[ks], W[ks][0], W[ks][1], sidx);
        }
        smf2<true>(ak0, ak1, af[0], W[10][0], W[10][1], sidx);
        smf2<true>(ak0, ak1, af[1], W[11][0], W[11][1], sidx);
        NOPS2(3, aq0, aq1);
        {
          const f32x4 z = pairsum4(aq0, aq1);
          ldsw64(lx + xw, u32x2{cvtpk(z[0], z[1]), cvtpk(z[2], z[3])});
        }
        BAR_LDS();
        if (__builtin_expect(s == 0 && w < 3, 0)) {
          poll_n<12>(rs, kq, [=](int) { return lane * 16; }, [=](int i) { return OFF_QK + h * 16384 + (i < 8 ? 8192 + i * 1024 : qoff + (i - 8) * 1024); }, budget);
        }
        bf16x8 qb[4];
        if (w == 0) {
#pragma unroll
          for (int ks = 0; ks < 4; ++ks) kq[8 + ks] = __builtin_bit_cast(u32x4, cat8(trread(lx + xr + ks * 512), trread(lx + xr + ks * 512 + 64)));
        }
#pragma unroll
        for (int ks = 0; ks < 4; ++ks) qb[ks] = asb8(kq[8 + ks]);
        SB();
#pragma unroll
        for (int ks = 2; ks < 10; ++ks) smf2<true>(ak0, ak1, af[ks], W[10 + ks][0], W[10 + ks][1], sidx);
        const f32x4 zero = {0.f, 0.f, 0.f, 0.f};
        f32x4 s1, s2;
        mf1<true>(s1, kq[0], qb[0]); mf1<true>(s2, kq[4], qb[0]);
        NOPS2(5, ak0, ak1);
        {
          const f32x4 z = pairsum4(ak0, ak1);
          ldsw64(lx + 2048 + xw, u32x2{cvtpk(z[0], z[1]), cvtpk(z[2], z[3])});
        }
        SB();
        mf1<false>(s1, kq[1], qb[1]); mf1<false>(s2, kq[5], qb[1]); mf1<false>(s1, kq[2], qb[2]);
        BAR_LDS();
        bf16x8 xk[4];
#pragma unroll
        for (int ks = 0; ks < 4; ++ks) xk[ks] = cat8(trread(lx + 2048 + xr + ks * 512), trread(lx + 2048 + xr + ks * 512 + 64));
        SB();
        mf1<false>(s2, kq[6], qb[2]); mf1<false>(s1, kq[3], qb[3]); mf1<false>(s2, kq[7], qb[3]);
        NOPS2(7, s1, s2);
        if (w == 0) {
          const bool hi8 = (lane & 8) != 0;
          f32x4 s0;
          const unsigned a = cvtpk(hi8 ? s2[0] : s1[0], hi8 ? s2[1] : s1[1]), c = cvtpk(hi8 ? s2[2] : s1[2], hi8 ? s2[3] : s1[3]);
          SB();
          mf1<true>(s0, __builtin_bit_cast(u32x4, xk[0]), qb[0]);
          const f32x2 sc2 = {SCALE, SCALE}, l2 = {LOG2E, LOG2E};
          const f32x2 a1 = f32x2{bflo(a), bfhi(a)} * sc2, c1 = f32x2{bflo(c), bfhi(c)} * sc2;
          SB();
          mf1<false>(s0, __builtin_bit_cast(u32x4, xk[1]), qb[1]);
          const unsigned a2 = cvtpk(a1[0], a1[1]), c2 = cvtpk(c1[0], c1[1]);
          const f32x2 t01 = {bflo(a2), bfhi(a2)}, t23 = {bflo(c2), bfhi(c2)};
          SB();
          mf1<false>(s0, __builtin_bit_cast(u32x4, xk[2]), qb[2]);
          float mx = vmax3(t01[0], t01[1], t23[0]);
          mx = vmax3(mx, mx, t23[1]);
          SB();
          mf1<false>(s0, __builtin_bit_cast(u32x4, xk[3]), qb[3]);
          f32x2 u01 = t01 * l2, u23 = t23 * l2;
          asm volatile("s_nop 5" : "+v"(s0), "+v"(u01), "+v"(u23));
          const float vm = rbf(rbf(lane >= 32 ? (hi8 ? s0[3] : s0[2]) : (hi8 ? s0[1] : s0[0])) * SCALE);
          RED8("v_max_f32", mx, vm);
          const float ml = mx * LOG2E;
          const f32x2 ml2 = {ml, ml};
          u01 -= ml2; u23 -= ml2;
          const float em = __builtin_amdgcn_exp2f(__builtin_fmaf(vm, LOG2E, -ml));
          const f32x2 e01 = {__builtin_amdgcn_exp2f(u01[0]), __builtin_amdgcn_exp2f(u01[1])}, e23 = {__builtin_amdgcn_exp2f(u23[0]), __builtin_amdgcn_exp2f(u23[1])};
          const f32x2 es = e01 + e23;
          float sum = es[0] + em;
          RED8("v_add_f32", sum, es[1]);
          const float inv = __builtin_amdgcn_rcpf(sum);
          const f32x2 inv2 = {inv, inv}, p01 = e01 * inv2, p23 = e23 * inv2;
          const float pmv = em * inv;
          u32x4 pt;
          pt[0] = nosent(cvtpk(p01[0], p01[1])); pt[1] = nosent(cvtpk(p23[0], p23[1]));
          pt[2] = ror8(pt[0]); pt[3] = ror8(pt[1]);
          const unsigned d0 = nosent(cvtpk(pmv, __uint_as_float(ror8(__float_as_uint(pmv)))));
          const unsigned dm = __builtin_amdgcn_update_dpp(d0, d0, 0x128, 0xF, 0xC, false);
          auto rm = __builtin_amdgcn_permlane32_swap(dm, dm, false, false);
          const unsigned po = OFF_R + s * REC + R_P + h * 640;
#pragma unroll
          for (int j = 0; j < RP / 2; ++j) st16(rs, (c16 >> 3) * 20480 + (c16 & 7) * 64 + g * 16, po + j * 40960, pt);
#pragma unroll
          for (int j = 0; j < RP / 4; ++j) st8(rs, ((c16 >> 3) + (g >> 1) * 2) * 20480 + 512 + (c16 & 7) * 16 + (g & 1) * 8, po + j * 81920, u32x2{rm[0], rm[1]});
          pace.mark(DA);
        } else {
          if (w < 3) {
            f32x4 s0 = MFMA16(xk[0], qb[0], zero);
            s0 = MFMA16(xk[1], qb[1], s0); s0 = MFMA16(xk[2], qb[2], s0); s0 = MFMA16(xk[3], qb[3], s0);
            float v[12];
#pragma unroll
            for (int i = 0; i < 4; ++i) {
              v[i] = g < 2 ? rbf(rbf(s0[i]) * SCALE) : -INFINITY;
              v[4 + i] = (4 * g + i <= lim) ? rbf(rbf(s1[i]) * SCALE) : -INFINITY;
              v[8 + i] = (16 + 4 * g + i <= lim) ? rbf(rbf(s2[i]) * SCALE) : -INFINITY;
            }
            float mx = v[0];
#pragma unroll
            for (int i = 1; i < 12; ++i) mx = fmaxf(mx, v[i]);
            mx = xmax32(xmax16(mx));
            float sum = 0.f;
#pragma unroll
            for (int i = 0; i < 12; ++i) { v[i] = __builtin_amdgcn_exp2f((v[i] - mx) * LOG2E); sum += v[i]; }
            sum = xsum32(xsum16(sum));
            const float inv = __builtin_amdgcn_rcpf(sum);
            u32x4 pt; u32x2 pm;
            pm[0] = nosent(cvtpk(v[0] * inv, v[1] * inv)); pm[1] = nosent(cvtpk(v[2] * inv, v[3] * inv));
            pt[0] = nosent(cvtpk(v[4] * inv, v[5] * inv)); pt[1] = nosent(cvtpk(v[6] * inv, v[7] * inv));
            pt[2] = nosent(cvtpk(v[8] * inv, v[9] * inv)); pt[3] = nosent(cvtpk(v[10] * inv, v[11] * inv));
            char* pd = rec + R_PT + h * 2560;
            st_sc1_x4(pd + (w - 1) * 1024 + lane * 16, pt);
            if (g < 2) st_sc1_x2(pd + 2048 + (16 * (w - 1) + c16) * 16 + g * 8, pm);
            pace.mark(DA - 15);
          } else pace.mark(DA + 25);
        }
        if (w < 3 && s + 1 < NSEG) fetch_qk(s + 1);
        if (s + 1 < NSEG) touch(rs, w == 0 ? R_P + (lane & 7) * 20480 + h * 640 : R_PT + h * 2560, OFF_R + (s + 1) * REC);
        drain_mem(lane);
        wait_fill(rs, s, budget);
        pace.pause();
      }
    } else {
      for (int s = 0; s < NSEG; ++s) {
        char* const rec = rec0 + (size_t)s * REC;
        f32x4 a0 = {0.f, 0.f, 0.f, 0.f}, a1 = a0;
        f32x4 c[5][2];
#pragma unroll
        for (int cb = 0; cb < 5; ++cb) { c[cb][0] = a0; c[cb][1] = a0; NOPS2(0, c[cb][0], c[cb][1]); }
        NOPS2(0, a0, a1);
        poll_mem(rec + R_MEM + (b % RM) * 10240, lmem, w, lane, budget);
        bf16x8 af[10];
#pragma unroll
        for (int ks = 0; ks < 10; ++ks) af[ks] = asb8(ldsr128o(lmem + ks * 1024 + lane * 16));
        __builtin_amdgcn_sched_barrier(0);
#pragma unroll
        for (int ks = 0; ks < 10; ++ks) {
          if (ks < NVS) smf2<false>(a0, a1, af[ks], W[ks][0], W[ks][1], sidx); else smf2<true>(a0, a1, af[ks], W[ks][0], W[ks][1], sidx);
        }
        NOPS2(7, a0, a1);
        {
          const f32x4 z = pairsum4(a0, a1);
          const u32x2 o = {cvtpk(z[0], z[1]), cvtpk(z[2], z[3])};
          ldsw64(lx + (32 * w + 16 * (lane >> 5) + c16) * 16 + (g & 1) * 8, o);
        }
        BAR_LDS();
        bf16x8 va[2];
#pragma unroll
        for (int ks = 0; ks < 2; ++ks) {
          const s16x4 x0 = trread(lx + (64 * ks + 16 * g + 8 * (p4 >> 1) + q4) * 16 + (p4 & 1) * 8);
          const s16x4 x1 = trread(lx + (64 * ks + 16 * g + 8 * (p4 >> 1) + 4 + q4) * 16 + (p4 & 1) * 8);
          va[ks] = cat8(x0, x1);
        }
        char* vd = rec + R_VW + (size_t)(2 * w + (lane >> 5)) * 8192 + h * 256 + c16 * 16 + (g & 1) * 8;
#pragma unroll
        for (int cb = 0; cb < 6; ++cb) {
          if (cb < 5) {
            smf2<true>(c[cb][0], c[cb][1], va[0], W[10 + 2 * cb][0], W[10 + 2 * cb][1], sidx);
            smf2<true>(c[cb][0], c[cb][1], va[1], W[11 + 2 * cb][0], W[11 + 2 * cb][1], sidx);
          }
          if (cb > 0) {
            f32x4 c0 = c[cb - 1][0], c1 = c[cb - 1][1];
            if (cb < 5) NOPS2(3, c0, c1); else NOPS2(7, c0, c1);
            const f32x4 z = pairsum4(c0, c1);
            const u32x2 o = {nosent(cvtpk(z[0], z[1])), nosent(cvtpk(z[2], z[3]))};
            st_sc1_x2(vd + (cb - 1) * 65536, o);
          }
        }
        pace.mark(DB);
        if (s + 1 < NSEG) touch(rs, R_VW + (2 * w + (lane >> 5)) * 8192 + h * 256 + min(lane & 7, 4) * 65536, OFF_R + (s + 1) * REC);
        drain_mem(lane);
        wait_fill(rs, s, budget);
        pace.pause();
      }
    }
  } else if (b < 104) {
    const int ct = b - 64;
    u32x4 uf[8];
    unsigned seen = 0;
    auto fetch_u = [&](int s) {
      wait_prog(rs, (32 + 8 * w + (lane & 7)) * 256, s + 1, seen, budget);
#pragma unroll
      for (int i = 0; i < 8; ++i) uf[i] = ld16(rs, lane * 16, OFF_U + ((s * 40 + ct) * 32 + 8 * w + i) * 1024);
    };
    const int r = lane & 7, hb = (lane >> 3) & 1;
#define PSLPC 1
#define CHK7 "s_sleep " STR(PSLPC) "\n\t" PEND_ASM("%[t]", "%[k5]", 248, 249, 250, 251) PEND_ASM("%[t]", "%[k6]", 252, 253, 254, 255) \
             PEND_ASM("%[t]", "%[k0]", 228, 229, 230, 231) "s_or_b64 %[x], %[k5], %[k6]\n\t" PEND_ASM("%[t]", "%[k1]", 232, 233, 234, 235) "s_or_b64 %[x], %[x], %[k0]\n\t" \
             PEND_ASM("%[t]", "%[k2]", 236, 237, 238, 239) "s_or_b64 %[x], %[x], %[k1]\n\t" PEND_ASM("%[t]", "%[k3]", 240, 241, 242, 243) "s_or_b64 %[x], %[x], %[k2]\n\t" \
             PEND_ASM("%[t]", "%[k4]", 244, 245, 246, 247) "s_or_b64 %[x], %[x], %[k3]\n\ts_or_b64 %[x], %[x], %[k4]\n\ts_cbranch_scc0 .Lcd%=\n\t"
#define CHK7N CHK7 CHK7 CHK7 CHK7
#define CHK7F CHK7 CHK7 CHK7
#define ROUND7 "s_mov_b64 exec, %[k5]\n\tglobal_load_dwordx4 v[248:251], %[vp], off sc1\n\t" \
               "s_mov_b64 exec, %[k6]\n\tglobal_load_dwordx4 v[252:255], %[vp], off offset:1024 sc1\n\t" \
               "s_mov_b64 exec, %[k0]\n\tglobal_load_dwordx4 v[228:231], %[pt], off sc1\n\t" \
               "s_mov_b64 exec, %[k1]\n\tglobal_load_dwordx4 v[232:235], %[pt], off offset:1280 sc1\n\t" \
               "s_mov_b64 exec, %[k2]\n\tglobal_load_dwordx4 v[236:239], %[pt], off offset:2560 sc1\n\t" \
               "s_mov_b64 exec, %[k3]\n\tglobal_load_dwordx4 v[240:243], %[pt], off offset:3840 sc1\n\t" \
               "s_mov_b64 exec, %[k4]\n\tglobal_load_dwordx4 v[244:247], %[pm], off sc1\n\t" \
               "s_mov_b64 exec, %[sv]\n\t"
#define M4(a, b, c, d) "v_mov_b32 v" #a ", -1\n\tv_mov_b32 v" #b ", -1\n\tv_mov_b32 v" #c ", -1\n\tv_mov_b32 v" #d ", -1\n\t"
#define DRAIN7() do { VM0(); asm volatile("s_waitcnt vmcnt(0)\n\t" M4(228, 229, 230, 231) M4(232, 233, 234, 235) M4(236, 237, 238, 239) M4(240, 241, 242, 243) \
                            M4(244, 245, 246, 247) M4(248, 249, 250, 251) M4(252, 253, 254, 255) ::: "memory", CLOB7); } while (0)
    const unsigned rpo = R_P + (ct % RP) * 20480;
    auto swap_u = [&]() {
      if (ct & 1) {
#pragma unroll
        for (int i = 0; i < 8; ++i) uf[i] = u32x4{uf[i][2], uf[i][3], uf[i][0], uf[i][1]};
      }
    };
    fetch_u(0);
    DRAIN7();
    swap_u();
    for (int s = 0; s < NSEG - 1; ++s) {
      char* const rec = rec0 + (size_t)s * REC;
      const unsigned lred = l0 + (s & 1) * 4096;
      const char* pt = rec + rpo + (8 * w + hb) * 640 + r * 64 + g * 16;
      const char* pm = rec + rpo + (8 * w + 4 * hb + g) * 640 + 512 + r * 16;
      const char* vp = rec + R_VW + (size_t)ct * 8192 + 8 * w * 256 + lane * 16;
      u64 k0, k1, k2, k3, k4, k5, k6, x, sv; unsigned tmp, bud = __builtin_amdgcn_readfirstlane(budget);
      unsigned ra = lred + (g * 8 + r) * 16;
      asm volatile("" : "+v"(ra));
      const unsigned wa = ra + hb * 2048 + w * 512;
      asm volatile("s_mov_b64 %[sv], exec\n\ts_mov_b64 %[k0], -1\n\ts_mov_b64 %[k1], -1\n\ts_mov_b64 %[k2], -1\n\ts_mov_b64 %[k3], -1\n\t"
                   "s_mov_b64 %[k4], -1\n\ts_mov_b64 %[k5], -1\n\ts_mov_b64 %[k6], -1\n"
                   ROUND7 CHK7N ROUND7 CHK7N
                   ".Lcl%=:\n\ts_waitcnt vmcnt(" STR(PCAPC) ")\n\t"
                   ROUND7 CHK7F
                   "s_sub_u32 %[bud], %[bud], 1\n\ts_cbranch_scc0 .Lcl%=\n\ts_mov_b32 %[bud], 0\n"
                   ".Lcd%=:\n\t"
                   "v_mfma_f32_16x16x32_bf16 v[220:223], %[u0], v[228:231], 0\n\tv_mfma_f32_16x16x32_bf16 v[224:227], %[u1], v[228:231], 0\n\t"
                   "v_mfma_f32_16x16x32_bf16 v[220:223], %[u2], v[232:235], v[220:223]\n\tv_mfma_f32_16x16x32_bf16 v[224:227], %[u3], v[232:235], v[224:227]\n\t"
                   "v_mfma_f32_16x16x32_bf16 v[220:223], %[u4], v[236:239], v[220:223]\n\tv_mfma_f32_16x16x32_bf16 v[224:227], %[u5], v[236:239], v[224:227]\n\t"
                   "v_mfma_f32_16x16x32_bf16 v[220:223], %[u6], v[240:243], v[220:223]\n\tv_mfma_f32_16x16x32_bf16 v[224:227], %[u7], v[240:243], v[224:227]\n\t"
                   "v_mfma_f32_16x16x32_bf16 v[220:223], v[248:251], v[244:247], v[220:223]\n\tv_mfma_f32_16x16x32_bf16 v[224:227], v[252:255], v[244:247], v[224:227]\n\t"
                   "s_nop 7\n\t"
                   "v_add_f32_dpp v220, v224, v220 row_ror:8 row_mask:0xf bank_mask:0xf\n\tv_add_f32_dpp v221, v225, v221 row_ror:8 row_mask:0xf bank_mask:0xf\n\t"
                   "v_add_f32_dpp v222, v226, v222 row_ror:8 row_mask:0xf bank_mask:0xf\n\tv_add_f32_dpp v223, v227, v223 row_ror:8 row_mask:0xf bank_mask:0xf\n\t"
                   "ds_write_b128 %[wa], v[220:223]\n\ts_waitcnt lgkmcnt(0)\n\ts_barrier"
                   : [k0] "=&s"(k0), [k1] "=&s"(k1), [k2] "=&s"(k2), [k3] "=&s"(k3), [k4] "=&s"(k4), [k5] "=&s"(k5), [k6] "=&s"(k6), [x] "=&s"(x),
                     [sv] "=&s"(sv), [t] "=&v"(tmp), [bud] "+s"(bud)
                   : [wa] "v"(wa), [pt] "v"(pt), [pm] "v"(pm), [vp] "v"(vp), [u0] "v"(uf[0]), [u1] "v"(uf[1]), [u2] "v"(uf[2]), [u3] "v"(uf[3]),
                     [u4] "v"(uf[4]), [u5] "v"(uf[5]), [u6] "v"(uf[6]), [u7] "v"(uf[7]) : "memory", "scc", CLOB7);
      budget = bud;
      {
        f32x4 z = __builtin_bit_cast(f32x4, ldsr128(ra)) + __builtin_bit_cast(f32x4, ldsr128(ra + 512));
        z += __builtin_bit_cast(f32x4, ldsr128(ra + 1024)) + __builtin_bit_cast(f32x4, ldsr128(ra + 1536));
        const u32x2 o = {nosent(cvtpk(z[0], z[1])), nosent(cvtpk(z[2], z[3]))};
#pragma unroll
        for (int j = 0; j < RM / 8; ++j)
          st8(rs, hb * 10240 + (g >> 1) * 128 + r * 16 + (g & 1) * 8, OFF_R + (s + 1) * REC + R_MEM + (w * (RM / 4) + 2 * j) * 10240 + ct * 256, o);
      }
      pace.mark(DC, PADDC);
      if (s > 1) delay_ticks(DLY_CU);
      if (s + 2 < NSEG) fetch_u(s + 1);
      if (s + 2 < NSEG) touch(rs, R_MEM + (2 * w + hb) * 10240 + ct * 256, OFF_R + (s + 2) * REC);
      DRAIN7();
      swap_u();
      wait_fill(rs, s, budget);
      pace.pause();
    }
  } else if (b < 168) {
    const bool qk = b < 136;
    const int h = (b - 104) & 31;
    bf16x8 W[20][2][2];
    const u16* const p0 = qk ? Wq : Wv; const u16* const p1 = qk ? Wk : Wo;
    auto xbuf = [=](int s) { return l0 + ((s & 1) ? 0u : 2 * XN_SEG); };
    auto stageX = [&](int s) {
#pragma unroll
      for (int j = 0; j < 10; ++j) glds16(ws + OFF_XN + (size_t)s * XN_SEG + (w * 10 + j) * 1024 + lane * 16, xbuf(s) + (w * 10 + j) * 1024);
    };
    load_slabs<20, false, 10>(l0, w, lane, 8 * g + q4, 4, 32 * w + 4 * p4, 16,
                   [=](int sl, int row, int ch) { return head_src(p0, p1, qk, h, sl, row, ch); },
                   [&](int sl, int tt, bf16x8 lo, bf16x8 hi) { W[sl][tt][0] = lo; W[sl][tt][1] = hi; }, [&] { stageX(0); });
    const unsigned lv = l0 + XN_SEG;
    const unsigned pword = OFF_FLG + (b - 104) * 256;
    for (int s = 0; s < NSEG; ++s) {
      if (s <= 4) asm volatile("s_waitcnt vmcnt(0)" ::: "memory");
      BAR_LDS();
      if (t == 0 && s >= 1) st4(rs, 0, pword, s <= 4 ? s : s - 1);
      if (s + 1 < NSEG) stageX(s + 1);
      const unsigned lxb = xbuf(s) + lane * 16;
      bf16x8 xa = asb8(ldsr128(lxb)), xb = asb8(ldsr128(lxb + 1024));
      if (qk) {
        f32x4 acc[2][4];
#pragma unroll
        for (int ks = 0; ks < 20; ++ks) {
          bf16x8 na = xa, nb = xb;
          if (ks + 1 < 20) { na = asb8(ldsr128(lxb + (ks + 1) * 2048)); nb = asb8(ldsr128(lxb + (ks + 1) * 2048 + 1024)); }
          __builtin_amdgcn_sched_barrier(0);
          const int sl = ks >> 1, kk = ks & 1;
#define QKSTEP(F) \
          if (sl < NVS) { mf2<false, true, F>(acc[0][0], acc[0][1], W[sl][0][kk], W[sl][1][kk], xa); mf2<false, true, F>(acc[1][0], acc[1][1], W[sl][0][kk], W[sl][1][kk], xb); } \
          else { mf2<true, true, F>(acc[0][0], acc[0][1], W[sl][0][kk], W[sl][1][kk], xa); mf2<true, true, F>(acc[1][0], acc[1][1], W[sl][0][kk], W[sl][1][kk], xb); } \
          mf2<true, true, F>(acc[0][2], acc[0][3], W[10 + sl][0][kk], W[10 + sl][1][kk], xa); mf2<true, true, F>(acc[1][2], acc[1][3], W[10 + sl][0][kk], W[10 + sl][1][kk], xb);
          if (ks == 0) { QKSTEP(true) } else { QKSTEP(false) }
          __builtin_amdgcn_sched_barrier(0);
          xa = na; xb = nb;
        }
        NOPS4(3, acc[0][0], acc[0][1], acc[0][2], acc[0][3]); NOPS4(3, acc[1][0], acc[1][1], acc[1][2], acc[1][3]);
        asm volatile("s_waitcnt vmcnt(0)" ::: "memory");
        const unsigned so = OFF_QK + (s * 32 + h) * 16384 + w * 1024, vo = (gp * 16 + c16) * 16;
#pragma unroll
        for (int mt = 0; mt < 2; ++mt) {
          st16(rs, vo, so + mt * 4096, nosent4(widen4(acc[mt][0], acc[mt][1])));
          st16(rs, vo, so + 8192 + mt * 4096, nosent4(widen4(acc[mt][2], acc[mt][3])));
        }
      } else {
        f32x4 av[2][2];
#pragma unroll
        for (int ks = 0; ks < 20; ++ks) {
          bf16x8 na = xa, nb = xb;
          if (ks + 1 < 20) { na = asb8(ldsr128(lxb + (ks + 1) * 2048)); nb = asb8(ldsr128(lxb + (ks + 1) * 2048 + 1024)); }
          __builtin_amdgcn_sched_barrier(0);
          const int sl = ks >> 1, kk = ks & 1;
#define VSTEP(F) \
          if (sl < NVS) { mf2<false, true, F>(av[0][0], av[0][1], W[sl][0][kk], W[sl][1][kk], xa); mf2<false, true, F>(av[1][0], av[1][1], W[sl][0][kk], W[sl][1][kk], xb); } \
          else { mf2<true, true, F>(av[0][0], av[0][1], W[sl][0][kk], W[sl][1][kk], xa); mf2<true, true, F>(av[1][0], av[1][1], W[sl][0][kk], W[sl][1][kk], xb); }
          if (ks == 0) { VSTEP(true) } else { VSTEP(false) }
          __builtin_amdgcn_sched_barrier(0);
          xa = na; xb = nb;
        }
        NOPS4(7, av[0][0], av[0][1], av[1][0], av[1][1]);
        ldsw128(lv + (w * 2) * 1024 + (gp * 16 + c16) * 16, widen4(av[0][0], av[0][1]));
        ldsw128(lv + (w * 2 + 1) * 1024 + (gp * 16 + c16) * 16, widen4(av[1][0], av[1][1]));
        BAR_LDS();
        bf16x8 vf[4][2];
#pragma unroll
        for (int i = 0; i < 8; ++i) vf[i >> 1][i & 1] = asb8(ldsr128(lv + i * 1024 + lane * 16));
        asm volatile("s_waitcnt vmcnt(0)" ::: "memory");
        __builtin_amdgcn_sched_barrier(0);
        f32x4 cu[5][2][2];
#pragma unroll
        for (int cb = 0; cb < 6; ++cb) {
          if (cb < 5) {
#pragma unroll
            for (int ks = 0; ks < 4; ++ks) {
              const int sl = 10 + 2 * cb + (ks >> 1), kk = ks & 1;
              if (ks == 0) { mf2<true, false, true>(cu[cb][0][0], cu[cb][0][1], W[sl][0][kk], W[sl][1][kk], vf[ks][0]); mf2<true, false, true>(cu[cb][1][0], cu[cb][1][1], W[sl][0][kk], W[sl][1][kk], vf[ks][1]); }
              else { mf2<true, false, false>(cu[cb][0][0], cu[cb][0][1], W[sl][0][kk], W[sl][1][kk], vf[ks][0]); mf2<true, false, false>(cu[cb][1][0], cu[cb][1][1], W[sl][0][kk], W[sl][1][kk], vf[ks][1]); }
            }
          }
          if (cb > 0) {
            f32x4 x0 = cu[cb - 1][0][0], x1 = cu[cb - 1][0][1], y0 = cu[cb - 1][1][0], y1 = cu[cb - 1][1][1];
            if (cb < 5) NOPS4(0, x0, x1, y0, y1); else NOPS4(7, x0, x1, y0, y1);
            const unsigned so = OFF_U + ((s * 40 + (cb - 1) * 8 + 2 * w) * 32 + h) * 1024;
            u32x4 o;
            o[0] = cvtpk(x0[0], x0[1]); o[1] = cvtpk(x0[2], x0[3]); o[2] = cvtpk(y0[0], y0[1]); o[3] = cvtpk(y0[2], y0[3]);
            st16(rs, lane * 16, so, o);
            o[0] = cvtpk(x1[0], x1[1]); o[1] = cvtpk(x1[2], x1[3]); o[2] = cvtpk(y1[0], y1[1]); o[3] = cvtpk(y1[2], y1[3]);
            st16(rs, lane * 16, so + 32768, o);
          }
        }
      }
    }
    BAR_ALL();
    if (t == 0) st4(rs, 0, pword, NSEG);
  }
  if (b >= 168 && b < 208) {
    const int ct = b - 168;
    u32x4 uf[8];
    unsigned seen = 0;
    auto fetch_u = [&](int s) {
      wait_prog(rs, (32 + 8 * w + (lane & 7)) * 256, s + 1, seen, budget);
#pragma unroll
      for (int i = 0; i < 8; ++i) uf[i] = ld16(rs, lane * 16, OFF_U + ((s * 40 + ct) * 32 + 8 * w + i) * 1024);
    };
    for (int s = 0; s < NSEG; ++s) {
      const unsigned ro = OFF_R + s * REC;
      u32x2 xr = {0u, 0u};
      if (w < 2) xr = *(const u32x2*)(x + (size_t)(s * 32 + 16 * w + c16) * D_ + ct * 16 + 4 * g);
      wait_word(rs, ro + R_PT + (8 * w + 7) * 2560 + 1024, budget);
      if (s == 0) fetch_u(0);
      u32x4 v[22];
      poll_n<22>(rs, v,
                 [=](int i) { return i < 16 ? lane * 16 : (i < 20 ? g * 2560 + c16 * 16 : lane * 16); },
                 [=](int i) { return i < 16 ? ro + R_PT + (8 * w + (i >> 1)) * 2560 + (i & 1) * 1024
                                            : (i < 20 ? ro + R_PT + (8 * w + 4 * ((i - 16) >> 1)) * 2560 + 2048 + (i & 1) * 256
                                                      : ro + R_VW + ct * 8192 + (8 * w + 4 * (i - 20)) * 256); }, budget);
      f32x4 a0 = {0.f, 0.f, 0.f, 0.f}, a1 = a0;
#pragma unroll
      for (int i = 0; i < 8; ++i) { a0 = MFMA16(asb8(uf[i]), asb8(v[2 * i]), a0); a1 = MFMA16(asb8(uf[i]), asb8(v[2 * i + 1]), a1); }
#pragma unroll
      for (int i = 0; i < 2; ++i) { a0 = MFMA16(asb8(v[20 + i]), asb8(v[16 + 2 * i]), a0); a1 = MFMA16(asb8(v[20 + i]), asb8(v[17 + 2 * i]), a1); }
      const unsigned lred = l0 + (s & 1) * 8192;
      ldsw128(lred + (w * 2) * 1024 + lane * 16, __builtin_bit_cast(u32x4, a0));
      ldsw128(lred + (w * 2 + 1) * 1024 + lane * 16, __builtin_bit_cast(u32x4, a1));
      BAR_LDS();
      if (w < 2) {
        f32x4 z = __builtin_bit_cast(f32x4, ldsr128(lred + w * 1024 + lane * 16));
#pragma unroll
        for (int i = 1; i < 4; ++i) z += __builtin_bit_cast(f32x4, ldsr128(lred + (i * 2 + w) * 1024 + lane * 16));
        u32x2 o;
        o[0] = nosent(cvtpk(bflo(xr[0]) + rbf(z[0]), bfhi(xr[0]) + rbf(z[1])));
        o[1] = nosent(cvtpk(bflo(xr[1]) + rbf(z[2]), bfhi(xr[1]) + rbf(z[3])));
        st8(rs, c16 * 32 + g * 8, ro + R_H + ct * 1024 + w * 512, o);
      }
      if (s + 1 < NSEG) fetch_u(s + 1);
      fill_share(rs, s, ct, t, ws);
      wait_fill(rs, s, budget);
    }
  } else if ((b >= 208 && b < 235) || (b >= 104 && b < 131)) {
    const int mt2 = b < 208 ? 1 : 0, j = b - (mt2 ? 104 : 208);
    bf16x8 W[20][2];
    load_slabs<10, false, 0>(l0, w, lane, 8 * g + q4, 4, 16 * w + 4 * p4, 64,
                   [=](int sl, int row, int ch) { return Wg + (ch < 8 ? (ptrdiff_t)0 : Wu - Wg) + (size_t)(sl * 64 + row) * F_ + 64 * j + (ch & 7) * 8; },
                   [&](int sl, int tt, bf16x8 lo, bf16x8 hi) { W[2 * sl][tt] = lo; W[2 * sl + 1][tt] = hi; }, [] {});
    const int row = 8 * w + (c16 >> 1), half = lane & 1;
    const unsigned hvo = g * 1024 + row * 32 + half * 16;
    u32x4 v[10];
#pragma unroll
    for (int i = 0; i < 10; ++i) v[i] = u32x4{~0u, ~0u, ~0u, ~0u};
    for (int s = mt2 ? SWG : 0; s < SWG; ++s) {
      const unsigned ro = OFF_R + s * REC;
      unsigned pend = 0;
#pragma unroll
      for (int i = 0; i < 10; ++i) pend |= (PENDING(v[i]) != 0 ? 1u : 0u) << i;
      if (pend == 0x3FFu) wait_word(rs, ro + R_H + w * 256, budget);
      if (pend) poll_n<10>(rs, v, [=](int) { return hvo; }, [=](int i) { return ro + R_H + i * 4096; }, budget, pend);
      ln_fast<10>(v, [](float z) { return xsum32(xsum16(xsum1(z))); });
      const unsigned lh = l0 + (s & 1) * XN_SEG;
#pragma unroll
      for (int i = 0; i < 10; ++i)
        ldsw128(lh + ((2 * i + (g >> 1)) * 2 + (row >> 4)) * 1024 + ((2 * (g & 1) + half) * 16 + (row & 15)) * 16, v[i]);
      wait_fill(rs, s, budget);
      if (s + 1 < SWG) {
#pragma unroll
        for (int i = 0; i < 10; ++i) v[i] = ld16(rs, hvo, ro + REC + R_H + i * 4096);
      }
      BAR_LDS();
      f32x4 acc[2][2];
      bf16x8 xf[20][2];
#pragma unroll
      for (int ks = 0; ks < 4; ++ks) { xf[ks][0] = asb8(ldsr128(lh + ks * 2048 + lane * 16)); xf[ks][1] = asb8(ldsr128(lh + ks * 2048 + 1024 + lane * 16)); }
#pragma unroll
      for (int ks = 0; ks < 20; ++ks) {
        if (ks + 4 < 20) { xf[ks + 4][0] = asb8(ldsr128(lh + (ks + 4) * 2048 + lane * 16)); xf[ks + 4][1] = asb8(ldsr128(lh + (ks + 4) * 2048 + 1024 + lane * 16)); }
        __builtin_amdgcn_sched_barrier(0);
        if (ks == 0) { mf2<true, true, true>(acc[0][0], acc[0][1], W[ks][0], W[ks][1], xf[ks][0]); mf2<true, true, true>(acc[1][0], acc[1][1], W[ks][0], W[ks][1], xf[ks][1]); }
        else { mf2<true, true, false>(acc[0][0], acc[0][1], W[ks][0], W[ks][1], xf[ks][0]); mf2<true, true, false>(acc[1][0], acc[1][1], W[ks][0], W[ks][1], xf[ks][1]); }
        __builtin_amdgcn_sched_barrier(0);
      }
      NOPS4(7, acc[0][0], acc[0][1], acc[1][0], acc[1][1]);
      f32x4 a[2];
#pragma unroll
      for (int mt = 0; mt < 2; ++mt)
#pragma unroll
        for (int i = 0; i < 4; ++i) { const float gt = rbf(acc[mt][0][i]), up = rbf(acc[mt][1][i]); a[mt][i] = (gt > 0.f ? gt : gt * 0.f) * up; }
      st16(rs, (g & 1) * 1024 + ((2 * (w & 1) + (g >> 1)) * 16 + c16) * 16, ro + R_ACT + (2 * j + (w >> 1)) * 2048, nosent4(widen4(a[0], a[1])));
    }
    if (mt2) wait_fill(rs, SWG - 3, budget);
    const int rr = 4 * w + q4;
    const unsigned hvo2 = g * 2048 + (p4 >> 1) * 1024 + (16 * mt2 + rr) * 32 + (p4 & 1) * 16;
    u32x4 u[5];
#pragma unroll
    for (int i = 0; i < 5; ++i) u[i] = u32x4{~0u, ~0u, ~0u, ~0u};
    for (int s = SWG; s < NSEG; ++s) {
      const unsigned ro = OFF_R + s * REC;
      unsigned pend = 0;
#pragma unroll
      for (int i = 0; i < 5; ++i) pend |= (PENDING(u[i]) != 0 ? 1u : 0u) << i;
      if (pend == 0x1Fu) wait_word(rs, ro + R_H + (16 * mt2 + 4 * w) * 32, budget);
      if (pend) poll_n<5>(rs, u, [=](int) { return hvo2; }, [=](int i) { return ro + R_H + i * 8192; }, budget, pend);
      ln_fast<5>(u, [](float z) { return xsum32(xsum16(xsum2(xsum1(z)))); });
      const unsigned lh = l0 + (s & 1) * XN_SEG;
#pragma unroll
      for (int i = 0; i < 5; ++i) ldsw128(lh + (4 * i + g) * 1024 + (p4 * 16 + rr) * 16, u[i]);
      wait_fill(rs, s, budget);
      if (s + 1 < NSEG) {
#pragma unroll
        for (int i = 0; i < 5; ++i) u[i] = ld16(rs, hvo2, ro + REC + R_H + i * 8192);
      }
      BAR_LDS();
      f32x4 ag, au;
      bf16x8 xf[20];
#pragma unroll
      for (int ks = 0; ks < 8; ++ks) xf[ks] = asb8(ldsr128(lh + ks * 1024 + lane * 16));
#pragma unroll
      for (int ks = 0; ks < 20; ++ks) {
        if (ks + 8 < 20) xf[ks + 8] = asb8(ldsr128(lh + (ks + 8) * 1024 + lane * 16));
        __builtin_amdgcn_sched_barrier(0);
        if (ks == 0) mf2<true, true, true>(ag, au, W[ks][0], W[ks][1], xf[ks]); else mf2<true, true, false>(ag, au, W[ks][0], W[ks][1], xf[ks]);
        __builtin_amdgcn_sched_barrier(0);
      }
      NOPS2(7, ag, au);
      float a[4];
#pragma unroll
      for (int i = 0; i < 4; ++i) { const float gt = rbf(ag[i]), up = rbf(au[i]); a[i] = (gt > 0.f ? gt : gt * 0.f) * up; }
      st8(rs, mt2 * 1024 + ((2 * (w & 1) + (g >> 1)) * 16 + c16) * 16 + (g & 1) * 8, ro + R_ACT + (2 * j + (w >> 1)) * 2048,
          u32x2{nosent(cvtpk(a[0], a[1])), nosent(cvtpk(a[2], a[3]))});
    }
  } else if ((b >= 235 && b < 255) || (b >= 136 && b < 156)) {
    const int mt2 = b < 235 ? 1 : 0, d = b - (mt2 ? 136 : 235);
#pragma unroll
    for (int i = 0; i < 27; ++i) glds16(Wd + (size_t)((w * 27 + i) * 16 + (lane >> 2)) * D_ + 32 * d + (lane & 3) * 8, l0 + (w * 27 + i) * 1024);
    BAR_ALL();
    const int k0 = w < 2 ? 14 * w : 13 * w + 2;
    bf16x8 W[14][2];
#pragma unroll
    for (int kk = 0; kk < 14; ++kk)
#pragma unroll
      for (int nt = 0; nt < 2; ++nt) {
        const int ks = (kk == 13 && w >= 2) ? k0 : k0 + kk;
        const unsigned a = l0 + (32 * ks + 8 * g + q4) * 64 + (16 * nt + 4 * p4) * 2;
        W[kk][nt] = cat8(trread(a), trread(a + 256));
      }
    BAR_LDS();
    for (int s = mt2 ? SWD : 0; s < SWD; ++s) {
      const unsigned ro = OFF_R + s * REC;
      wait_word(rs, ro + R_ACT + k0 * 2048, budget);
      u32x4 v[28];
      poll_n<28>(rs, v, [=](int) { return lane * 16; }, [=](int i) { return ro + R_ACT + ((i >= 26 && w >= 2) ? k0 * 2 + (i - 26) : k0 * 2 + i) * 1024; }, budget);
      u32x4 hr = {0u, 0u, 0u, 0u};
      if (w < 2) hr = ld16(rs, (g & 1) * 1024 + c16 * 32 + (g >> 1) * 16, ro + R_H + 2 * d * 1024 + w * 512);
      f32x4 acc[2][2];
#pragma unroll
      for (int kk = 0; kk < 13; ++kk) {
        if (kk == 0) { mf2<true, true, true>(acc[0][0], acc[0][1], W[kk][0], W[kk][1], asb8(v[2 * kk])); mf2<true, true, true>(acc[1][0], acc[1][1], W[kk][0], W[kk][1], asb8(v[2 * kk + 1])); }
        else { mf2<true, true, false>(acc[0][0], acc[0][1], W[kk][0], W[kk][1], asb8(v[2 * kk])); mf2<true, true, false>(acc[1][0], acc[1][1], W[kk][0], W[kk][1], asb8(v[2 * kk + 1])); }
      }
      if (w < 2) { mf2<true, true, false>(acc[0][0], acc[0][1], W[13][0], W[13][1], asb8(v[26])); mf2<true, true, false>(acc[1][0], acc[1][1], W[13][0], W[13][1], asb8(v[27])); }
      NOPS4(7, acc[0][0], acc[0][1], acc[1][0], acc[1][1]);
      const unsigned lred = l0 + (s & 1) * 16384;
#pragma unroll
      for (int i = 0; i < 4; ++i) ldsw128(lred + (w * 4 + i) * 1024 + lane * 16, __builtin_bit_cast(u32x4, acc[i >> 1][i & 1]));
      BAR_LDS();
      if (w < 2) {
        f32x4 z0 = __builtin_bit_cast(f32x4, ldsr128(lred + (2 * w) * 1024 + lane * 16)), z1 = __builtin_bit_cast(f32x4, ldsr128(lred + (2 * w + 1) * 1024 + lane * 16));
#pragma unroll
        for (int i = 1; i < 4; ++i) {
          z0 += __builtin_bit_cast(f32x4, ldsr128(lred + (i * 4 + 2 * w) * 1024 + lane * 16));
          z1 += __builtin_bit_cast(f32x4, ldsr128(lred + (i * 4 + 2 * w + 1) * 1024 + lane * 16));
        }
        const u32x4 o = widen4(z0, z1);
        u32x4 z;
#pragma unroll
        for (int i = 0; i < 4; ++i) z[i] = cvtpk(bflo(hr[i]) + bflo(o[i]), bfhi(hr[i]) + bfhi(o[i]));
        *(u32x4*)(out + (size_t)(s * 32 + 16 * w + c16) * D_ + 32 * d + 16 * (g & 1) + 8 * (g >> 1)) = z;
      }
      fill_share(rs, s, 40 + d, t, ws);
      wait_fill(rs, s, budget);
    }
    if (mt2) wait_fill(rs, SWD - 3, budget);
    for (int s = SWD; s < NSEG; ++s) {
      const unsigned ro = OFF_R + s * REC + mt2 * 1024;
      wait_word(rs, ro + R_ACT + k0 * 2048, budget);
      u32x4 v[14];
      poll_n<14>(rs, v, [=](int) { return lane * 16; }, [=](int i) { return ro + R_ACT + ((i == 13 && w >= 2) ? k0 : k0 + i) * 2048; }, budget);
      u32x4 hr = {0u, 0u, 0u, 0u};
      if (w == 0) hr = ld16(rs, (g & 1) * 1024 + c16 * 32 + (g >> 1) * 16, ro + R_H + 2 * d * 1024 - mt2 * 512);
      f32x4 a0, a1;
#pragma unroll
      for (int kk = 0; kk < 13; ++kk) {
        if (kk == 0) mf2<true, true, true>(a0, a1, W[kk][0], W[kk][1], asb8(v[kk])); else mf2<true, true, false>(a0, a1, W[kk][0], W[kk][1], asb8(v[kk]));
      }
      if (w < 2) mf2<true, true, false>(a0, a1, W[13][0], W[13][1], asb8(v[13]));
      NOPS2(7, a0, a1);
      const unsigned lred = l0 + (s & 1) * 16384;
      ldsw128(lred + (w * 2) * 1024 + lane * 16, __builtin_bit_cast(u32x4, a0));
      ldsw128(lred + (w * 2 + 1) * 1024 + lane * 16, __builtin_bit_cast(u32x4, a1));
      BAR_LDS();
      if (w == 0) {
        f32x4 z0 = __builtin_bit_cast(f32x4, ldsr128(lred + lane * 16)), z1 = __builtin_bit_cast(f32x4, ldsr128(lred + 1024 + lane * 16));
#pragma unroll
        for (int i = 1; i < 4; ++i) {
          z0 += __builtin_bit_cast(f32x4, ldsr128(lred + (i * 2) * 1024 + lane * 16));
          z1 += __builtin_bit_cast(f32x4, ldsr128(lred + (i * 2 + 1) * 1024 + lane * 16));
        }
        const u32x4 o = widen4(z0, z1);
        u32x4 z;
#pragma unroll
        for (int i = 0; i < 4; ++i) z[i] = cvtpk(bflo(hr[i]) + bflo(o[i]), bfhi(hr[i]) + bfhi(o[i]));
        *(u32x4*)(out + (size_t)(s * 32 + 16 * mt2 + c16) * D_ + 32 * d + 16 * (g & 1) + 8 * (g >> 1)) = z;
      }
      wait_fill(rs, s, budget);
    }
  }
}

extern "C" void kernel_launch(void* const* d_in, const int* in_sizes, int n_in, void* d_out, int out_size, void* d_ws, size_t ws_size,
                              hipStream_t stream) {
  const u16* x = (const u16*)d_in[0];
  char* ws = (char*)d_ws;
  prep_kernel<<<128 + FILL_BLOCKS + 1, 256, 0, stream>>>(x, (const u16*)d_in[1], ws);
  mega_kernel<<<256, 256, 0, stream>>>(x, (const u16*)d_in[2], (const u16*)d_in[3], (const u16*)d_in[4], (const u16*)d_in[5],
                                       (const u16*)d_in[8], (const u16*)d_in[9], (const u16*)d_in[10], (u16*)d_out, ws);
}
```
